```python
import torch

def _softmax(x, axis=-1):
    return torch.softmax(x, dim=axis)
def _gelu(x):
    return torch.nn.functional.gelu(x, approximate="tanh")
def _silu(x):
    return torch.nn.functional.silu(x)
def _sigmoid(x):
    return torch.sigmoid(x)
def _layernorm(x, axis=-1, eps=1e-5):
    m = x.mean(dim=axis, keepdim=True)
    v = x.var(dim=axis, keepdim=True, unbiased=False)
    return (x - m) / torch.sqrt(v + eps)
def _scatter_add(tgt, idx, src, axis=0):
    return tgt.clone().index_add_(axis, idx, src)
def _segment_sum(src, seg, n):
    out = torch.zeros((n,) + src.shape[1:], dtype=src.dtype)
    return out.index_add_(0, seg, src)
def _pad(x, pw):
    flat = tuple(v for pair in reversed(pw) for v in pair)
    return torch.nn.functional.pad(x, flat)

kernel_name = 'concat_heads__gqa_block_cached__mha_block_cached__decode'

def setup_inputs():

    _w = (lambda a: (lambda t: t.to(torch.bfloat16) if t.is_floating_point() else t)(torch.as_tensor(a)))
    def _A_setup():
        B, Sq, D = 1, 1, 448
        Hq, Hkv, Sk, Dh = 16, 8, 32768, 256
        assert Hq % Hkv == 0, (Hq, Hkv)
        return {
            "x": _w((lambda *s: torch.randn(*s, dtype=torch.bfloat16) * 0.8)(B, Sq, D)),
            "k_cache": _w((lambda *s: torch.randn(*s, dtype=torch.bfloat16) * 1.6)(B, Hkv, Sk, Dh)),
            "v_cache": _w((lambda *s: torch.randn(*s, dtype=torch.bfloat16) * 0.2)(B, Hkv, Sk, Dh)),
            "wq": _w((lambda *s: torch.randn(*s, dtype=torch.bfloat16) * 1.6)(D, Hq * Dh) * (D ** -0.5)),
            "wo": _w((lambda *s: torch.randn(*s, dtype=torch.bfloat16) * 0.2)(Hq * Dh, D) * ((Hq * Dh) ** -0.5)),
        }
    _A_in = _A_setup()

    def _B_setup():
        B, Sq, D = 1, 1, 448
        H, Sk, Dh = 16, 32768, 256
        return {
            "x": _w((lambda *s: torch.randn(*s, dtype=torch.bfloat16) * 0.8)(B, Sq, D)),
            "k_cache": _w((lambda *s: torch.randn(*s, dtype=torch.bfloat16) * 1.6)(B, H, Sk, Dh)),
            "v_cache": _w((lambda *s: torch.randn(*s, dtype=torch.bfloat16) * 0.2)(B, H, Sk, Dh)),
            "wq": _w((lambda *s: torch.randn(*s, dtype=torch.bfloat16) * 1.6)(D, H * Dh) * (D ** -0.5)),
            "wo": _w((lambda *s: torch.randn(*s, dtype=torch.bfloat16) * 0.2)(H * Dh, D) * ((H * Dh) ** -0.5)),
        }
    _B_in = _B_setup()

    return {
        "A_x": _A_in["x"],
        "A_k_cache": _A_in["k_cache"],
        "A_v_cache": _A_in["v_cache"],
        "A_wq": _A_in["wq"],
        "A_wo": _A_in["wo"],
        "B_x": _B_in["x"],
        "B_k_cache": _B_in["k_cache"],
        "B_v_cache": _B_in["v_cache"],
        "B_wq": _B_in["wq"],
        "B_wo": _B_in["wo"],
    }

def reference(A_x, A_k_cache, A_v_cache, A_wq, A_wo, B_x, B_k_cache, B_v_cache, B_wq, B_wo):
    def _A_ref(x, k_cache, v_cache, wq, wo):
        B, Sq, D = x.shape
        Hq, Dh = 16, 256
        Hkv = k_cache.shape[1]
        rep = Hq // Hkv
        q = (lambda x, *d: x.permute(*d))((x @ wq).reshape(B, Sq, Hq, Dh), 0, 2, 1, 3)
        k = (lambda x, n, axis: torch.repeat_interleave(x, n, dim=axis))(k_cache, rep, axis=1)
        v = (lambda x, n, axis: torch.repeat_interleave(x, n, dim=axis))(v_cache, rep, axis=1)
        scores = torch.einsum("bhqd,bhkd->bhqk", q, k) * (Dh ** -0.5)
        p = _softmax(scores, axis=-1)
        o = torch.einsum("bhqk,bhkd->bhqd", p, v)
        return (lambda x, *d: x.permute(*d))(o, 0, 2, 1, 3).reshape(B, Sq, Hq * Dh) @ wo

    def _B_ref(x, k_cache, v_cache, wq, wo):
        B, Sq, D = x.shape
        H, Dh = 16, 256
        q = (lambda x, *d: x.permute(*d))((x @ wq).reshape(B, Sq, H, Dh), 0, 2, 1, 3)
        scores = torch.einsum("bhqd,bhkd->bhqk", q, k_cache) * (Dh ** -0.5)
        p = _softmax(scores, axis=-1)
        o = torch.einsum("bhqk,bhkd->bhqd", p, v_cache)
        return (lambda x, *d: x.permute(*d))(o, 0, 2, 1, 3).reshape(B, Sq, H * Dh) @ wo

    a = _A_ref(A_x, A_k_cache, A_v_cache, A_wq, A_wo)
    b = _B_ref(B_x, B_k_cache, B_v_cache, B_wq, B_wo)
    return torch.concatenate([a, b], axis=-1)
```

```cpp
#include <hip/hip_runtime.h>
#include <hip/hip_bf16.h>
#include <stdint.h>

#define GAS __attribute__((address_space(1)))
#define LAS __attribute__((address_space(3)))
typedef unsigned int u32;
typedef unsigned long long u64;
typedef u32 u32x4 __attribute__((ext_vector_type(4)));
typedef u32 u32x2 __attribute__((ext_vector_type(2)));
typedef float f32x4 __attribute__((ext_vector_type(4)));
typedef __bf16 bf16x8 __attribute__((ext_vector_type(8)));
typedef __bf16 bf16x2 __attribute__((ext_vector_type(2)));
#define RLX_AGENT __ATOMIC_RELAXED, __HIP_MEMORY_SCOPE_AGENT

constexpr int DM = 448;
constexpr int SK = 32768;
constexpr int NWG = 256;
#define NGRP 4
constexpr int WPG = NWG / NGRP;
constexpr int NA = 8 / NGRP, NB = 16 / NGRP;
constexpr int NPH = NA + NB;
constexpr int NQS = 2 * NA + NB;
constexpr int TILE_B = 8192;
constexpr int TPH = SK / 16;
constexpr int TPP = TPH / WPG;
#define TCH 16
constexpr int NT = NPH * TPP;
#define NCONS 2
#define NSLOT 9
constexpr int SLOT_B = 2 * TILE_B;
constexpr int NTHREADS = 64 * (NCONS + 2);
constexpr float SCL = 0.0625f * 1.4426950408889634f;
static_assert(TPP % TCH == 0 && NT == 192, "tiling");

__host__ __device__ constexpr bool ph_isA(int p) { return p >= NB / 2 && p < NB / 2 + NA; }
__host__ __device__ constexpr int ph_kv(int p, int g) { return ph_isA(p) ? g * NA + (p - NB / 2) : g * NB + (p < NB / 2 ? p : p - NA); }
__host__ __device__ constexpr int ph_slot(int p) { return p <= NB / 2 ? p : (p < NB / 2 + NA ? NB / 2 + 2 * (p - NB / 2) : p + NA); }
__host__ __device__ constexpr int slot_qh(int s, int g) { return s < NB / 2 ? 16 + g * NB + s : (s < NB / 2 + 2 * NA ? g * 2 * NA + (s - NB / 2) : 16 + g * NB + (s - 2 * NA)); }

constexpr int L_RING = 0;
constexpr int L_Q = NSLOT * SLOT_B;
constexpr int MREC = 260;
constexpr int L_MRG = L_Q + NQS * 512;
constexpr int L_Y = L_MRG + NCONS * 2 * MREC * 4;
constexpr int L_CTL = L_Y + DM * 4;
constexpr int L_END = L_CTL + 64;
static_assert(L_END <= 163840, "LDS budget");
constexpr int C_FULL = 0, C_DONE = 1  , C_QRDY = 4  , C_DUMP = 5, C_MERGED = 6, C_ABORT = 7;

constexpr size_t W_EPOCH = 0;
constexpr size_t W_QG = 4096;
constexpr int PREC_B = 1152;
constexpr size_t W_PART = W_QG + 8192 * 8;
constexpr size_t W_Y = W_PART + (size_t)32 * WPG * PREC_B;
constexpr size_t W_DBG = W_Y + (size_t)2 * 128 * DM * 8;
constexpr size_t W_END = W_DBG + NWG * 32 * 8;

struct Params {
  const unsigned short *xA, *kA, *vA, *wqA, *woA, *xB, *kB, *vB, *wqB, *woB;
  unsigned short* out;
  unsigned char* ws;
};

__device__ __forceinline__ float xrow16_max(float x) {
  auto s = __builtin_amdgcn_permlane16_swap(__float_as_uint(x), __float_as_uint(x), false, false);
  x = fmaxf(__uint_as_float(s[0]), __uint_as_float(s[1]));
  auto t = __builtin_amdgcn_permlane32_swap(__float_as_uint(x), __float_as_uint(x), false, false);
  return fmaxf(__uint_as_float(t[0]), __uint_as_float(t[1]));
}
__device__ __forceinline__ float xrow16_sum(float x) {
  auto s = __builtin_amdgcn_permlane16_swap(__float_as_uint(x), __float_as_uint(x), false, false);
  x = __uint_as_float(s[0]) + __uint_as_float(s[1]);
  auto t = __builtin_amdgcn_permlane32_swap(__float_as_uint(x), __float_as_uint(x), false, false);
  return __uint_as_float(t[0]) + __uint_as_float(t[1]);
}
__device__ __forceinline__ u32 pack_bf16(float lo, float hi) {
  bf16x2 v = {(__bf16)lo, (__bf16)hi};
  return __builtin_bit_cast(u32, v);
}
__device__ __forceinline__ float bf_lo(u32 v) { return __uint_as_float(v << 16); }
__device__ __forceinline__ float bf_hi(u32 v) { return __uint_as_float(v & 0xFFFF0000u); }
__device__ __forceinline__ float dot2(u32 a, u32 b, float acc) {
  return __builtin_amdgcn_fdot2_f32_bf16(__builtin_bit_cast(bf16x2, a), __builtin_bit_cast(bf16x2, b), acc, false);
}
__device__ __forceinline__ f32x4 mfma16(u32x4 a, u32x4 b, f32x4 c) {
  return __builtin_amdgcn_mfma_f32_16x16x32_bf16(__builtin_bit_cast(bf16x8, a), __builtin_bit_cast(bf16x8, b), c, 0, 0, 0);
}
__device__ __forceinline__ u64 mix64(u64 z) {
  z += 0x9E3779B97F4A7C15ull;
  z = (z ^ (z >> 30)) * 0xBF58476D1CE4E5B9ull;
  z = (z ^ (z >> 27)) * 0x94D049BB133111EBull;
  return z ^ (z >> 31);
}
__device__ __forceinline__ u64 rtime() { return __builtin_amdgcn_s_memrealtime(); }
constexpr long long SPIN_CAP = 20000000;
__device__ __forceinline__ u32x4 ld_nt(const void* p) { return __builtin_nontemporal_load((const GAS u32x4*)p); }

#define LDS_U32(off) ((volatile LAS u32*)((LAS unsigned char*)lds + (off)))
#define GLDS(src, dst) __builtin_amdgcn_global_load_lds((const GAS u32*)(src), (LAS u32*)(dst), 16, 0, 2)

#define STAMP(i) do { } while (0)

__device__ __forceinline__ bool lds_wait_ge(volatile LAS u32* w, u32 need, volatile LAS u32* abortw, bool sleep) {
  if (*w >= need) return true;
  const u64 t0 = rtime();
  for (u32 sp = 1;; ++sp) {
    if (*w >= need) return true;
    if (sleep) __builtin_amdgcn_s_sleep(1);
    if ((sp & 255u) == 0u) {
      if (*abortw != 0u) return false;
      if ((long long)(rtime() - t0) > SPIN_CAP) { *abortw = 1u; return false; }
    }
  }
}
__device__ __forceinline__ bool poll_expired(u32 sp, u64 t0, volatile LAS u32* abortw) {
  if ((sp & 63u) == 0u && (long long)(rtime() - t0) > SPIN_CAP) { *abortw = 1u; return true; }
  return false;
}

#define INFL 32
#define STR2(x) #x
#define STR(x) STR2(x)
#define THROTTLE() asm volatile("s_waitcnt vmcnt(" STR(INFL) ")" ::: "memory")

__global__ void __launch_bounds__(NTHREADS) fused_decode_kernel(Params P) {
  extern __shared__ __attribute__((aligned(16))) unsigned char lds[];
  const int tid = threadIdx.x, lane = tid & 63;
  const int wave = __builtin_amdgcn_readfirstlane(tid >> 6);
  const int wg = blockIdx.x, grp = wg & (NGRP - 1), rank = wg / NGRP;
  volatile LAS u32* ctl = LDS_U32(L_CTL);

  if (wave == 0) {
    if (lane < 16) ctl[lane] = 0u;
    asm volatile("s_waitcnt lgkmcnt(0)" ::: "memory");
  }
  __builtin_amdgcn_s_barrier();

  if (wave == 0) {
    const unsigned koff = (lane & 15) * 512 + (lane >> 4) * 16;
    const unsigned voff = (lane >> 4) * 2048 + (lane & 15) * 16;
    STAMP(0);
    u32 published = 0;
    bool ok = true;
    int slot = 0;
#pragma unroll 1
    for (int p = 0; p < NPH; ++p) {
      const size_t hoff = (size_t)ph_kv(p, grp) * SK * 512;
      const unsigned char* kb = (const unsigned char*)(ph_isA(p) ? P.kA : P.kB) + hoff + koff;
      const unsigned char* vb = (const unsigned char*)(ph_isA(p) ? P.vA : P.vB) + hoff + voff;
#pragma unroll 1
      for (int i = 0; i < TPP; ++i) {
        const int n = p * TPP + i;
        const int tile = (i / TCH) * (TCH * WPG) + rank * TCH + (i % TCH);
        const unsigned char* kp = kb + (size_t)tile * TILE_B;
        const unsigned char* vp = vb + (size_t)tile * TILE_B;
        if (n >= NSLOT && ok) {
          volatile LAS u32* dn = ctl + C_DONE + (n - NSLOT) % NCONS;
          const u32 need = n - NSLOT + 1;
          if (*dn < need) {
            asm volatile("s_waitcnt vmcnt(0)" ::: "memory");
            published = n; ctl[C_FULL] = published;
            ok = lds_wait_ge(dn, need, ctl + C_ABORT, true);
          }
          asm volatile("" ::: "memory");
        }
        unsigned char* dst = lds + L_RING + slot * SLOT_B;
#pragma unroll
        for (int h = 0; h < 4; ++h) {
#pragma unroll
          for (int j = 0; j < 4; ++j) {
            const int jj = (h & 1) * 4 + j;
            GLDS(h < 2 ? kp + jj * 64 : vp + (jj >> 1) * 512 + (jj & 1) * 256, dst + (h * 4 + j) * 1024);
          }
          THROTTLE();
          if ((INFL - 4 * (h + 1)) % 16 == 0) {
            const int landed = n - (INFL - 4 * (h + 1)) / 16;
            if (landed > (int)published) { published = landed; ctl[C_FULL] = published; }
          }
        }
        slot = (slot + 1 == NSLOT) ? 0 : slot + 1;
      }
    }
    STAMP(2);
    if (INFL > 48) { asm volatile("s_waitcnt vmcnt(48)" ::: "memory"); if (published < NT - 3) { published = NT - 3; ctl[C_FULL] = NT - 3; } }
    if (INFL > 32) { asm volatile("s_waitcnt vmcnt(32)" ::: "memory"); if (published < NT - 2) { published = NT - 2; ctl[C_FULL] = NT - 2; } }
    if (INFL > 16) { asm volatile("s_waitcnt vmcnt(16)" ::: "memory"); if (published < NT - 1) { published = NT - 1; ctl[C_FULL] = NT - 1; } }
    asm volatile("s_waitcnt vmcnt(0)" ::: "memory");
    ctl[C_FULL] = NT;
    STAMP(1);
  } else if (wave <= NCONS) {
    const int c = wave - 1;
    const int cc = lane & 15, g = lane >> 4;
    u32 mask[4];
#pragma unroll
    for (int q = 0; q < 4; ++q) mask[q] = (q == ((cc & 7) >> 1)) ? ((cc & 1) ? 0xFFFF0000u : 0x0000FFFFu) : 0u;
    bool ok = true;
    int n = c;
#pragma unroll 1
    for (int p = 0; p < NPH; ++p) {
      const bool isA = ph_isA(p);
      const int s0i = ph_slot(p);
      if (ok) ok = lds_wait_ge(ctl + C_QRDY, (u32)(s0i + (isA ? 2 : 1)), ctl + C_ABORT, true);
      asm volatile("" ::: "memory");
      if (p == 0 && c == 0) STAMP(3);
      const int qslot = s0i + (isA ? (cc >> 3) : 0);
      u32x4 qf[8];
#pragma unroll
      for (int s = 0; s < 8; ++s) qf[s] = *(const LAS u32x4*)((LAS unsigned char*)lds + L_Q + qslot * 512 + s * 64 + g * 16);
      float m = -INFINITY, l = 0.f;
      f32x4 o0 = {0.f, 0.f, 0.f, 0.f}, o1 = {0.f, 0.f, 0.f, 0.f};
      const int nend = (p + 1) * TPP;
#pragma unroll 1
      for (; n < nend; n += NCONS) {
        if (ok) ok = lds_wait_ge(ctl + C_FULL, (u32)(n + 1), ctl + C_ABORT, false);
        asm volatile("" ::: "memory");
        const LAS unsigned char* sb = (const LAS unsigned char*)lds + L_RING + (n % NSLOT) * SLOT_B + lane * 16;
        u32x4 kf[8], vf[8];
#pragma unroll
        for (int j = 0; j < 8; ++j) kf[j] = *(const LAS u32x4*)(sb + j * 1024);
#pragma unroll
        for (int j = 0; j < 8; ++j) vf[j] = *(const LAS u32x4*)(sb + 8192 + j * 1024);
        f32x4 s0 = {0.f, 0.f, 0.f, 0.f}, s1 = {0.f, 0.f, 0.f, 0.f};
#pragma unroll
        for (int j = 0; j < 8; j += 2) { s0 = mfma16(kf[j], qf[j], s0); s1 = mfma16(kf[j + 1], qf[j + 1], s1); }
        asm volatile("s_waitcnt lgkmcnt(0)" ::: "memory");
        ctl[C_DONE + c] = (u32)(n + 1);
        const f32x4 s = s0 + s1;
        float t = fmaxf(fmaxf(s[0], s[1]), fmaxf(s[2], s[3]));
        t = xrow16_max(t);
        const float mn = fmaxf(m, t * SCL);
        const float alpha = __builtin_amdgcn_exp2f(m - mn);
        m = mn;
        float pr[4];
#pragma unroll
        for (int r = 0; r < 4; ++r) pr[r] = __builtin_amdgcn_exp2f(fmaf(s[r], SCL, -mn));
        l = fmaf(l, alpha, (pr[0] + pr[1]) + (pr[2] + pr[3]));
        o0 *= alpha; o1 *= alpha;
#pragma unroll
        for (int r = 0; r < 4; ++r) {
          const u32 pk = pack_bf16(pr[r], pr[r]);
          const u32x4 b = {pk & mask[0], pk & mask[1], pk & mask[2], pk & mask[3]};
          o0 = mfma16(vf[2 * r], b, o0);
          o1 = mfma16(vf[2 * r + 1], b, o1);
        }
      }
      const float lt = xrow16_sum(l);
      if (p > 0 && ok) ok = lds_wait_ge(ctl + C_MERGED, (u32)p, ctl + C_ABORT, true);
      asm volatile("" ::: "memory");
      const int hd = cc >> 3;
      if (isA || hd == 0) {
        LAS float* r = (LAS float*)((LAS unsigned char*)lds + L_MRG) + (c * 2 + hd) * MREC;
#pragma unroll
        for (int k = 0; k < 4; ++k) { r[32 * g + 8 * k + (cc & 7)] = o0[k]; r[128 + 32 * g + 8 * k + (cc & 7)] = o1[k]; }
        if (g == 0 && (cc & 7) == 0) { r[256] = m; r[257] = lt; }
      }
      asm volatile("s_waitcnt lgkmcnt(0)" ::: "memory");
      if (lane == 0) __hip_atomic_fetch_add((LAS u32*)(ctl + C_DUMP), 1u, __ATOMIC_RELAXED, __HIP_MEMORY_SCOPE_WORKGROUP);
    }
    if (c == 0) STAMP(4);
  } else {
    GAS u64* epoch_p = (GAS u64*)(P.ws + W_EPOCH);
    const u64 e0 = __hip_atomic_load(epoch_p, RLX_AGENT);
    const int prob = wg >> 7, w7 = wg & 127;
    const int sl = ((w7 >> 4) << 4) + ((w7 & 7) << 1) + ((w7 >> 3) & 1);
    const int a = lane >> 2, ch = lane & 3;
    const unsigned char* wq = (const unsigned char*)(prob ? P.wqB : P.wqA) + (size_t)(28 * a) * 8192 + sl * 64 + ch * 16;
    const unsigned char* xp = (const unsigned char*)(prob ? P.xB : P.xA) + a * 56;
    u32x4 wv[28];
    u32x2 xv[7];
#pragma unroll
    for (int i = 0; i < 7; ++i) xv[i] = __builtin_nontemporal_load((const GAS u32x2*)(xp + i * 8));
#pragma unroll
    for (int i = 0; i < 28; ++i) wv[i] = ld_nt(wq + (size_t)i * 8192);
    STAMP(5);
    const u64 tag = mix64(e0);
    const u32 tag32 = (u32)(tag >> 32);
    float acc[8];
#pragma unroll
    for (int k = 0; k < 8; ++k) acc[k] = 0.f;
#pragma unroll
    for (int i = 0; i < 14; ++i) {
      const u32 xx = xv[i >> 1][i & 1];
#pragma unroll
      for (int d = 0; d < 4; ++d) {
        const u32 w0 = wv[2 * i][d], w1 = wv[2 * i + 1][d];
        acc[2 * d] = dot2(__builtin_amdgcn_perm(w1, w0, 0x05040100u), xx, acc[2 * d]);
        acc[2 * d + 1] = dot2(__builtin_amdgcn_perm(w1, w0, 0x07060302u), xx, acc[2 * d + 1]);
      }
    }
#pragma unroll
    for (int k = 0; k < 8; ++k) {
      float v = acc[k];
      v += __shfl_xor(v, 4); v += __shfl_xor(v, 8); v += __shfl_xor(v, 16); v += __shfl_xor(v, 32);
      acc[k] = v;
    }
    {
      float v = acc[0];
#pragma unroll
      for (int k = 1; k < 8; ++k) v = (a == k) ? acc[k] : v;
      const u32 bits = pack_bf16(v, 0.f) & 0xFFFFu;
      if (a < 8) __hip_atomic_store((GAS u64*)(P.ws + W_QG) + prob * 4096 + sl * 32 + ch * 8 + a, (tag & ~0xFFFFull) | bits, RLX_AGENT);
    }
    STAMP(6);
    bool ok = true;
#pragma unroll 1
    for (int qs = 0; qs < NQS; ++qs) {
      const int qh = slot_qh(qs, grp);
      GAS u64* gq = (GAS u64*)(P.ws + W_QG) + qh * 256 + lane;
      u64 v[4];
      const u64 t0 = rtime();
      for (u32 sp = 1; ok; ++sp) {
        bool good = true;
#pragma unroll
        for (int i = 0; i < 4; ++i) { v[i] = __hip_atomic_load(gq + 64 * i, RLX_AGENT); good &= ((v[i] ^ tag) >> 16) == 0; }
        if (__all(good)) break;
        __builtin_amdgcn_s_sleep(4);
        if (poll_expired(sp, t0, ctl + C_ABORT)) ok = false;
      }
      LAS unsigned short* qd = (LAS unsigned short*)((LAS unsigned char*)lds + L_Q + qs * 512);
#pragma unroll
      for (int i = 0; i < 4; ++i) qd[lane + 64 * i] = (unsigned short)v[i];
      asm volatile("s_waitcnt lgkmcnt(0)" ::: "memory");
      ctl[C_QRDY] = (u32)(qs + 1);
      if (qs == 0) STAMP(7);
    }
    STAMP(8);
    const int uq = wg >> 3, us = wg & 7;
    const int col = lane < 56 ? lane : 55;
    u32x4 wo[32];
    {
      const unsigned char* wp = (const unsigned char*)(uq >= 16 ? P.woB : P.woA) + (size_t)((uq & 15) * 256 + us * 32) * 896 + col * 16;
#pragma unroll
      for (int k = 0; k < 32; ++k) wo[k] = ld_nt(wp + k * 896);
#pragma unroll
      for (int kp = 0; kp < 16; ++kp)
#pragma unroll
        for (int d = 0; d < 4; ++d) {
          const u32 w0 = wo[2 * kp][d], w1 = wo[2 * kp + 1][d];
          wo[2 * kp][d] = __builtin_amdgcn_perm(w1, w0, 0x05040100u);
          wo[2 * kp + 1][d] = __builtin_amdgcn_perm(w1, w0, 0x07060302u);
        }
    }
    u32x2 rec[NQS];
    float recm[NQS], recl[NQS];
#pragma unroll
    for (int p = 0; p < NPH; ++p) {
      if (ok) ok = lds_wait_ge(ctl + C_DUMP, (u32)(NCONS * (p + 1)), ctl + C_ABORT, true);
      asm volatile("" ::: "memory");
      if (p == NPH - 1) STAMP(9);
#pragma unroll
      for (int h = 0; h < (ph_isA(p) ? 2 : 1); ++h) {
        const LAS float* r = (const LAS float*)((LAS unsigned char*)lds + L_MRG) + h * MREC;
        float mm[NCONS], M = -INFINITY;
#pragma unroll
        for (int c = 0; c < NCONS; ++c) { mm[c] = r[c * 2 * MREC + 256]; M = fmaxf(M, mm[c]); }
        float L = 0.f; f32x4 o = {0.f, 0.f, 0.f, 0.f};
#pragma unroll
        for (int c = 0; c < NCONS; ++c) {
          const float w = __builtin_amdgcn_exp2f(mm[c] - M);
          L = fmaf(w, r[c * 2 * MREC + 257], L);
          o += w * *(const LAS f32x4*)(r + c * 2 * MREC + 4 * lane);
        }
        const int qs = ph_slot(p) + h;
        rec[qs] = u32x2{pack_bf16(o[0], o[1]), pack_bf16(o[2], o[3])};
        recm[qs] = M; recl[qs] = L;
      }
      asm volatile("s_waitcnt lgkmcnt(0)" ::: "memory");
      ctl[C_MERGED] = (u32)(p + 1);
    }
    const __amdgpu_buffer_rsrc_t part_rs = __builtin_amdgcn_make_buffer_rsrc(P.ws + W_PART, 0, 32 * WPG * PREC_B, 0x00020000);
    const __amdgpu_buffer_rsrc_t y_rs = __builtin_amdgcn_make_buffer_rsrc(P.ws + W_Y, 0, 2 * 128 * DM * 8, 0x00020000);
#pragma unroll
    for (int qs = NQS - 1; qs >= 0; --qs) {
      const int ro = (slot_qh(qs, grp) * WPG + rank) * PREC_B;
      const u32x4 gr = {rec[qs][0], tag32, rec[qs][1], tag32};
      __builtin_amdgcn_raw_buffer_store_b128(gr, part_rs, ro + lane * 16, 0, 16);
      if (lane == 0) {
        const u32x4 ml = {__float_as_uint(recm[qs]), tag32, __float_as_uint(recl[qs]), tag32};
        __builtin_amdgcn_raw_buffer_store_b128(ml, part_rs, ro + 1024, 0, 16);
      }
    }
    STAMP(10);
    constexpr int NLD = WPG / 8;
    constexpr int NML = (WPG + 63) / 64;
    u32x4 pv[NLD], ml[NML];
    {
      const int rbase = uq * WPG * PREC_B;
      const u64 t0 = rtime();
      for (u32 sp = 1; ok; ++sp) {
        bool good = true;
#pragma unroll
        for (int i = 0; i < NML; ++i) {
          const int j = (i * 64 + lane) < WPG ? (i * 64 + lane) : WPG - 1;
          ml[i] = __builtin_amdgcn_raw_buffer_load_b128(part_rs, rbase + j * PREC_B + 1024, 0, 16);
        }
#pragma unroll
        for (int i = 0; i < NLD; ++i)
          pv[i] = __builtin_amdgcn_raw_buffer_load_b128(part_rs, rbase + (i * 8 + (lane >> 3)) * PREC_B + us * 128 + (lane & 7) * 16, 0, 16);
#pragma unroll
        for (int i = 0; i < NML; ++i) good &= (ml[i][1] == tag32) & (ml[i][3] == tag32);
#pragma unroll
        for (int i = 0; i < NLD; ++i) good &= (pv[i][1] == tag32) & (pv[i][3] == tag32);
        if (__all(good)) break;
        __builtin_amdgcn_s_sleep(1);
        if (poll_expired(sp, t0, ctl + C_ABORT)) ok = false;
      }
    }
    STAMP(11);
    float y[8];
    {
      float M = -INFINITY;
#pragma unroll
      for (int i = 0; i < NML; ++i) M = fmaxf(M, (i * 64 + lane < WPG) ? __uint_as_float(ml[i][0]) : -INFINITY);
#pragma unroll
      for (int s = 1; s < 64; s <<= 1) M = fmaxf(M, __shfl_xor(M, s));
      float w[NML], L = 0.f;
#pragma unroll
      for (int i = 0; i < NML; ++i) {
        w[i] = (i * 64 + lane < WPG) ? __builtin_amdgcn_exp2f(__uint_as_float(ml[i][0]) - M) : 0.f;
        L = fmaf(w[i], __uint_as_float(ml[i][2]), L);
      }
#pragma unroll
      for (int s = 1; s < 64; s <<= 1) L += __shfl_xor(L, s);
      float o[4] = {0.f, 0.f, 0.f, 0.f};
#pragma unroll
      for (int i = 0; i < NLD; ++i) {
        const float wj = __shfl(w[(i * 8) / 64], ((i * 8) & 63) + (lane >> 3));
        o[0] = fmaf(wj, bf_lo(pv[i][0]), o[0]); o[1] = fmaf(wj, bf_hi(pv[i][0]), o[1]);
        o[2] = fmaf(wj, bf_lo(pv[i][2]), o[2]); o[3] = fmaf(wj, bf_hi(pv[i][2]), o[3]);
      }
      const float inv = 1.f / L;
#pragma unroll
      for (int k = 0; k < 4; ++k) {
        float v = o[k];
        v += __shfl_xor(v, 8); v += __shfl_xor(v, 16); v += __shfl_xor(v, 32);
        o[k] = v * inv;
      }
      const u32 op0 = pack_bf16(o[0], o[1]), op1 = pack_bf16(o[2], o[3]);
#pragma unroll
      for (int k = 0; k < 8; ++k) y[k] = 0.f;
#pragma unroll
      for (int kp = 0; kp < 16; ++kp) {
        const u32 ov = __builtin_amdgcn_readlane((kp & 1) ? op1 : op0, kp >> 1);
#pragma unroll
        for (int d = 0; d < 4; ++d) {
          y[2 * d] = dot2(wo[2 * kp][d], ov, y[2 * d]);
          y[2 * d + 1] = dot2(wo[2 * kp + 1][d], ov, y[2 * d + 1]);
        }
      }
    }
    {
      LAS float* yl = (LAS float*)((LAS unsigned char*)lds + L_Y);
      if (lane < 56) {
        *(LAS f32x4*)(yl + lane * 8) = f32x4{y[0], y[1], y[2], y[3]};
        *(LAS f32x4*)(yl + lane * 8 + 4) = f32x4{y[4], y[5], y[6], y[7]};
      }
      asm volatile("s_waitcnt lgkmcnt(0)" ::: "memory");
      const int yo = (uq >> 4) * 128 * DM * 8 + (wg & 127) * DM * 8;
#pragma unroll
      for (int i = 0; i < 4; ++i) {
        const int c2 = i * 128 + lane * 2;
        if (c2 < DM) {
          const u32x4 gr = {__float_as_uint(yl[c2]), tag32, __float_as_uint(yl[c2 + 1]), tag32};
          __builtin_amdgcn_raw_buffer_store_b128(gr, y_rs, yo + c2 * 8, 0, 16);
        }
      }
    }
    STAMP(12);
    if (wg < 56) {
      const int pr = wg / 28, fw = wg % 28;
      u32x4 t[16];
      const u64 t0 = rtime();
      for (u32 sp = 1; ok; ++sp) {
        bool good = true;
#pragma unroll
        for (int i = 0; i < 16; ++i)
          t[i] = __builtin_amdgcn_raw_buffer_load_b128(y_rs, (pr * 128 + i * 8 + (lane >> 3)) * DM * 8 + fw * 128 + (lane & 7) * 16, 0, 16);
#pragma unroll
        for (int i = 0; i < 16; ++i) good &= (t[i][1] == tag32) & (t[i][3] == tag32);
        if (__all(good)) break;
        __builtin_amdgcn_s_sleep(1);
        if (poll_expired(sp, t0, ctl + C_ABORT)) ok = false;
      }
      float s0 = 0.f, s1 = 0.f;
#pragma unroll
      for (int i = 0; i < 16; ++i) { s0 += __uint_as_float(t[i][0]); s1 += __uint_as_float(t[i][2]); }
      s0 += __shfl_xor(s0, 8); s0 += __shfl_xor(s0, 16); s0 += __shfl_xor(s0, 32);
      s1 += __shfl_xor(s1, 8); s1 += __shfl_xor(s1, 16); s1 += __shfl_xor(s1, 32);
      if (lane < 8) *(GAS u32*)(P.out + pr * DM + fw * 16 + lane * 2) = pack_bf16(s0, s1);
      if (wg == 28 && lane == 0) __hip_atomic_store(epoch_p, tag, RLX_AGENT);
      STAMP(13);
    }
  }
}

extern "C" void kernel_launch(void* const* d_in, const int* in_sizes, int n_in, void* d_out, int out_size, void* d_ws, size_t ws_size,
                              hipStream_t stream) {
  (void)in_sizes; (void)n_in; (void)out_size; (void)ws_size;
  Params p;
  p.xA = (const unsigned short*)d_in[0]; p.kA = (const unsigned short*)d_in[1]; p.vA = (const unsigned short*)d_in[2];
  p.wqA = (const unsigned short*)d_in[3]; p.woA = (const unsigned short*)d_in[4];
  p.xB = (const unsigned short*)d_in[5]; p.kB = (const unsigned short*)d_in[6]; p.vB = (const unsigned short*)d_in[7];
  p.wqB = (const unsigned short*)d_in[8]; p.woB = (const unsigned short*)d_in[9];
  p.out = (unsigned short*)d_out; p.ws = (unsigned char*)d_ws;
  (void)hipFuncSetAttribute((const void*)fused_decode_kernel, hipFuncAttributeMaxDynamicSharedMemorySize, L_END);
  hipLaunchKernelGGL(fused_decode_kernel, dim3(NWG), dim3(NTHREADS), L_END, stream, p);
}
```

```cpp
#include <hip/hip_runtime.h>
#include <hip/hip_bf16.h>
#include <stdint.h>

#define GAS __attribute__((address_space(1)))
#define LAS __attribute__((address_space(3)))
typedef unsigned int u32;
typedef unsigned long long u64;
typedef u32 u32x4 __attribute__((ext_vector_type(4)));
typedef u32 u32x2 __attribute__((ext_vector_type(2)));
typedef float f32x4 __attribute__((ext_vector_type(4)));
typedef __bf16 bf16x8 __attribute__((ext_vector_type(8)));
typedef __bf16 bf16x2 __attribute__((ext_vector_type(2)));
#define RLX_AGENT __ATOMIC_RELAXED, __HIP_MEMORY_SCOPE_AGENT

constexpr int DM = 448;
constexpr int SK = 32768;
constexpr int NWG = 256;
#define NGRP 4
constexpr int WPG = NWG / NGRP;
constexpr int NA = 8 / NGRP, NB = 16 / NGRP;
constexpr int NPH = NA + NB;
constexpr int NQS = 2 * NA + NB;
constexpr int TILE_B = 8192;
constexpr int TPH = SK / 16;
constexpr int TPP = TPH / WPG;
#define TCH (2 * NGRP)
constexpr int NT = NPH * TPP;
constexpr int NCONS = 2;
#define NSLOT (NQS > 16 ? 8 : 9)
#define PB 4
constexpr int SLOT_B = 2 * TILE_B;
constexpr int NTHREADS = 64 * (NCONS + 2);
constexpr float SCL = 0.0625f * 1.4426950408889634f;
static_assert(TPP % TCH == 0 && NT == 192 && TPP >= 2 * PB, "tiling");

__host__ __device__ constexpr bool ph_isA(int p) { return p >= NB / 2 && p < NB / 2 + NA; }
__host__ __device__ constexpr int ph_kv(int p, int g) { return ph_isA(p) ? g * NA + (p - NB / 2) : g * NB + (p < NB / 2 ? p : p - NA); }
__host__ __device__ constexpr int ph_slot(int p) { return p <= NB / 2 ? p : (p < NB / 2 + NA ? NB / 2 + 2 * (p - NB / 2) : p + NA); }
__host__ __device__ constexpr int slot_qh(int s, int g) { return s < NB / 2 ? 16 + g * NB + s : (s < NB / 2 + 2 * NA ? g * 2 * NA + (s - NB / 2) : 16 + g * NB + (s - 2 * NA)); }
static_assert(!ph_isA(NPH - 1), "the last phase carries one query head");

constexpr int L_RING = 0;
constexpr int L_Q = NSLOT * SLOT_B;
constexpr int L_ML = L_Q + NQS * 512;
constexpr int MRG_B = 2048 + 512;
constexpr int L_MRG = L_ML + NQS * 8;
constexpr int L_CTL = L_MRG + NCONS * MRG_B;
constexpr int L_END = L_CTL + 64;
static_assert(L_END <= 163840, "LDS budget");
constexpr int C_FULL = 0, C_DONE = 1  , C_QRDY = 4  , C_DUMP = 5  , C_ABORT = 7, C_TAG = 8, C_REG = 9,
              C_MERGED = 10, C_PUB = 11, C_GCNT = 12, C_T0 = 13, C_YPUB = 14, C_FCNT = 15;
constexpr int L_WP = L_RING;
constexpr int L_FP = L_RING + 1024;

constexpr int NREG = 16;
constexpr u32 R_QG = 0;
constexpr u32 R_PART = R_QG + 4096 * 8;
constexpr u32 R_ML = R_PART + 32 * 8 * WPG * 128;
constexpr u32 R_Y = R_ML + 32 * WPG * 16;
constexpr u32 REG_B = (R_Y + 2 * 28 * 128 * 128 + 4095) / 4096 * 4096;
constexpr size_t W_EPOCH = 0;
constexpr size_t W_DBG = 4096;
constexpr size_t W_REG = W_DBG + NWG * 32 * 8;
constexpr size_t WS_BYTES = W_REG + (size_t)NREG * REG_B;
constexpr u32 OOB = 0x7FFFFFF0u;

struct Params {
  const unsigned short *xA, *kA, *vA, *wqA, *woA, *xB, *kB, *vB, *wqB, *woB;
  unsigned short* out;
  unsigned char* ws;
};

template <int CTRL> __device__ __forceinline__ float dpp(float x) {
  return __builtin_bit_cast(float, __builtin_amdgcn_mov_dpp(__builtin_bit_cast(int, x), CTRL, 0xf, 0xf, true));
}
constexpr int D_XOR1 = 0xB1, D_XOR2 = 0x4E, D_ROR4 = 0x124, D_ROR8 = 0x128;
__device__ __forceinline__ float xrow16_max(float x) {
  auto s = __builtin_amdgcn_permlane16_swap(__float_as_uint(x), __float_as_uint(x), false, false);
  x = fmaxf(__uint_as_float(s[0]), __uint_as_float(s[1]));
  auto t = __builtin_amdgcn_permlane32_swap(__float_as_uint(x), __float_as_uint(x), false, false);
  return fmaxf(__uint_as_float(t[0]), __uint_as_float(t[1]));
}
__device__ __forceinline__ float xrow16_sum(float x) {
  auto s = __builtin_amdgcn_permlane16_swap(__float_as_uint(x), __float_as_uint(x), false, false);
  x = __uint_as_float(s[0]) + __uint_as_float(s[1]);
  auto t = __builtin_amdgcn_permlane32_swap(__float_as_uint(x), __float_as_uint(x), false, false);
  return __uint_as_float(t[0]) + __uint_as_float(t[1]);
}
__device__ __forceinline__ float oct_max(float x) {
  return xrow16_max(fmaxf(x, dpp<D_ROR8>(x)));
}
__device__ __forceinline__ float oct_sum(float x) {
  return xrow16_sum(x + dpp<D_ROR8>(x));
}
__device__ __forceinline__ u32 pack_bf16(float lo, float hi) {
  bf16x2 v = {(__bf16)lo, (__bf16)hi};
  return __builtin_bit_cast(u32, v);
}
__device__ __forceinline__ float bf_lo(u32 v) { return __uint_as_float(v << 16); }
__device__ __forceinline__ float bf_hi(u32 v) { return __uint_as_float(v & 0xFFFF0000u); }
__device__ __forceinline__ float round_bf16(float x) { return bf_lo(pack_bf16(x, x)); }
__device__ __forceinline__ float dot2(u32 a, u32 b, float acc) {
  return __builtin_amdgcn_fdot2_f32_bf16(__builtin_bit_cast(bf16x2, a), __builtin_bit_cast(bf16x2, b), acc, false);
}
__device__ __forceinline__ f32x4 mfma16(u32x4 a, u32x4 b, f32x4 c) {
  return __builtin_amdgcn_mfma_f32_16x16x32_bf16(__builtin_bit_cast(bf16x8, a), __builtin_bit_cast(bf16x8, b), c, 0, 0, 0);
}
__device__ __forceinline__ u64 mix64(u64 z) {
  z += 0x9E3779B97F4A7C15ull;
  z = (z ^ (z >> 30)) * 0xBF58476D1CE4E5B9ull;
  z = (z ^ (z >> 27)) * 0x94D049BB133111EBull;
  return z ^ (z >> 31);
}
__device__ __forceinline__ u64 rtime() { return __builtin_amdgcn_s_memrealtime(); }
__device__ __forceinline__ void pause(long long ticks) {
  const u64 t0 = rtime();
  while ((long long)(rtime() - t0) < ticks) __builtin_amdgcn_s_sleep(1);
}
constexpr long long SPIN_CAP = 20000000;
__device__ __forceinline__ u32x4 ld_nt(const void* p) { return __builtin_nontemporal_load((const GAS u32x4*)p); }
__device__ __forceinline__ bool tagged(u32x4 v, u32 tag) { return (v[1] == tag) & (v[3] == tag); }

#define LDSP(T, off) ((LAS T*)((LAS unsigned char*)lds + (off)))
#define LGKM0() asm volatile("s_waitcnt lgkmcnt(0)" ::: "memory")
#define ST_SC1(v, rs, off) __builtin_amdgcn_raw_buffer_store_b128((v), (rs), (off), 0, 16)
#define LD_SC1(rs, off) __builtin_amdgcn_raw_buffer_load_b128((rs), (off), 0, 16)

#define STAMP(i) do { } while (0)

template <int SLEEP>
__device__ __forceinline__ bool lds_wait_ge(volatile LAS u32* w, u32 need, volatile LAS u32* abortw) {
  if (*w >= need) return true;
  const u64 t0 = rtime();
  for (u32 sp = 1;; ++sp) {
    if (*w >= need) return true;
    if (SLEEP) __builtin_amdgcn_s_sleep(SLEEP);
    if ((sp & 255u) == 0u) {
      if (*abortw != 0u) return false;
      if ((long long)(rtime() - t0) > SPIN_CAP) { *abortw = 1u; return false; }
    }
  }
}
__device__ __forceinline__ bool poll_expired(u32 sp, u64 t0, volatile LAS u32* abortw) {
  if ((sp & 63u) == 0u && (long long)(rtime() - t0) > SPIN_CAP) { *abortw = 1u; return true; }
  return false;
}

#define KV_AUX 2
#define INFL 48
#define ROT 1
#define PACE 14533
#define BURST 4
#define OLD_LEAD 2
#define OLD_GATE 100
#define POLL_GATE 200
#define CONS_SLEEP 1
#define DELAY1 10
#define REPOLL 0
#define DELAY2 30
#define WAITVM(n) asm volatile("s_waitcnt vmcnt(%0)" :: "i"(n) : "memory")
#define PIECE(src, goff, loff) __builtin_amdgcn_global_load_lds((const GAS u32*)(src), (LAS u32*)((LAS unsigned char*)lds + dst + (loff) - (goff)), 16, (goff), KV_AUX)
template <int WIN>
__device__ __forceinline__ void issue_tile(const unsigned char* kp, const unsigned char* vp, unsigned char* lds, u32 dst, int n, u32& published, volatile LAS u32* ctl) {
#define AFTER(h) do { WAITVM(WIN); if ((WIN - 4 * ((h) + 1)) % 16 == 0) {         \
    const int landed = n - (WIN - 4 * ((h) + 1)) / 16; if (landed > (int)published) { published = landed; ctl[C_FULL] = published; } } } while (0)
  PIECE(kp, 0, 0); PIECE(kp, 64, 1024); PIECE(kp, 128, 2048); PIECE(kp, 192, 3072);
  AFTER(0);
  PIECE(kp, 256, 4096); PIECE(kp, 320, 5120); PIECE(kp, 384, 6144); PIECE(kp, 448, 7168);
  AFTER(1);
  PIECE(vp, 0, 8192); PIECE(vp, 256, 9216); PIECE(vp, 512, 10240); PIECE(vp, 768, 11264);
  AFTER(2);
  PIECE(vp, 1024, 12288); PIECE(vp, 1280, 13312); PIECE(vp, 1536, 14336); PIECE(vp, 1792, 15360);
  AFTER(3);
#undef AFTER
}

struct Tile { u32x4 k[8], v[8]; };
struct Acc { float m, l; f32x4 o0, o1; };

__device__ __forceinline__ void tile_read(Tile& t, const LAS unsigned char* sb) {
#pragma unroll
  for (int j = 0; j < 8; ++j) t.k[j] = *(const LAS u32x4*)(sb + j * 1024);
#pragma unroll
  for (int j = 0; j < 8; ++j) t.v[j] = *(const LAS u32x4*)(sb + 8192 + j * 1024);
}
__device__ __forceinline__ void tile_math(const Tile& t, const u32x4 (&qf)[8], const u32 (&mask)[4], Acc& a) {
  f32x4 s0 = {0.f, 0.f, 0.f, 0.f}, s1 = {0.f, 0.f, 0.f, 0.f};
#pragma unroll
  for (int j = 0; j < 8; j += 2) { s0 = mfma16(t.k[j], qf[j], s0); s1 = mfma16(t.k[j + 1], qf[j + 1], s1); }
  float s[4];
#pragma unroll
  for (int r = 0; r < 4; ++r) s[r] = round_bf16(s0[r] + s1[r]);
  float mx = fmaxf(fmaxf(s[0], s[1]), fmaxf(s[2], s[3]));
  mx = xrow16_max(mx);
  const float mn = fmaxf(a.m, mx * SCL);
  const float alpha = __builtin_amdgcn_exp2f(a.m - mn);
  a.m = mn;
  float pr[4];
#pragma unroll
  for (int r = 0; r < 4; ++r) pr[r] = __builtin_amdgcn_exp2f(fmaf(s[r], SCL, -mn));
  a.l = fmaf(a.l, alpha, (pr[0] + pr[1]) + (pr[2] + pr[3]));
  a.o0 *= alpha; a.o1 *= alpha;
#pragma unroll
  for (int r = 0; r < 4; ++r) {
    const u32 pk = pack_bf16(pr[r], pr[r]);
    const u32x4 b = {pk & mask[0], pk & mask[1], pk & mask[2], pk & mask[3]};
    a.o0 = mfma16(t.v[2 * r], b, a.o0);
    a.o1 = mfma16(t.v[2 * r + 1], b, a.o1);
  }
}

template <int N, class F>
__device__ __forceinline__ bool gather_granules(const __amdgpu_buffer_rsrc_t rs, u32 tag, u32x4 (&v)[N], F off, bool ok, volatile LAS u32* abortw) {
#pragma unroll
  for (int i = 0; i < N; ++i) v[i] = LD_SC1(rs, off(i));
  const u64 t0 = rtime();
  for (u32 sp = 1; ok; ++sp) {
    bool good = true;
#pragma unroll
    for (int i = 0; i < N; ++i) good &= tagged(v[i], tag);
    if (__all(good)) break;
    if (REPOLL) pause(REPOLL);
#pragma unroll
    for (int i = 0; i < N; ++i) {
      const bool g = tagged(v[i], tag);
      const u32x4 t = LD_SC1(rs, g ? OOB : off(i));
      v[i] = g ? v[i] : t;
    }
    if (poll_expired(sp, t0, abortw)) ok = false;
  }
  return ok;
}

#define NGW_MIN 64
constexpr int NGW = WPG >= NGW_MIN ? 4 : 1;
constexpr int NPW = WPG / NGW;
constexpr int NLD = NPW / 8;
__device__ __forceinline__ bool gather_part(const __amdgpu_buffer_rsrc_t rs, u32 tag, int uq, int us, int j0, int lane, bool ok,
                                            volatile LAS u32* abortw, float& Mo, float& Lo, float (&oo)[4]) {
  u32x4 g[2 * NLD];
  const u32 pb = R_PART + ((uq * 8 + us) * WPG + j0) * 128 + lane * 16, mb = R_ML + (uq * WPG + j0 + (lane >> 3)) * 16;
  ok = gather_granules(rs, tag, g, [&](int i) { return (i & 1) ? mb + (i >> 1) * 128 : pb + (i >> 1) * 1024; }, ok, abortw);
  float M = -INFINITY;
#pragma unroll
  for (int i = 0; i < NLD; ++i) M = fmaxf(M, __uint_as_float(g[2 * i + 1][0]));
  M = oct_max(M);
  float L = 0.f, o[4] = {0.f, 0.f, 0.f, 0.f};
#pragma unroll
  for (int i = 0; i < NLD; ++i) {
    const float w = __builtin_amdgcn_exp2f(__uint_as_float(g[2 * i + 1][0]) - M);
    L = fmaf(w, __uint_as_float(g[2 * i + 1][2]), L);
    o[0] = fmaf(w, bf_lo(g[2 * i][0]), o[0]); o[1] = fmaf(w, bf_hi(g[2 * i][0]), o[1]);
    o[2] = fmaf(w, bf_lo(g[2 * i][2]), o[2]); o[3] = fmaf(w, bf_hi(g[2 * i][2]), o[3]);
  }
  Mo = M; Lo = oct_sum(L);
#pragma unroll
  for (int k = 0; k < 4; ++k) oo[k] = oct_sum(o[k]);
  return ok;
}

constexpr int FW = 4;
__device__ __forceinline__ bool final_share(const __amdgpu_buffer_rsrc_t rs, u32 tag, int wg, int w, int lane, bool ok, volatile LAS u32* abortw,
                                            float& s0, float& s1) {
  constexpr int NL = 16 / FW;
  const u32 yb = R_Y + wg * 128 * 128 + w * NL * 1024 + lane * 16;
  u32x4 t[NL];
  ok = gather_granules(rs, tag, t, [&](int i) { return yb + i * 1024; }, ok, abortw);
  float a0 = 0.f, a1 = 0.f;
#pragma unroll
  for (int i = 0; i < NL; ++i) { a0 += __uint_as_float(t[i][0]); a1 += __uint_as_float(t[i][2]); }
  s0 = oct_sum(a0); s1 = oct_sum(a1);
  return ok;
}

#define POLL_WAIT() do { if (PACE && POLL_GATE > -100000) { const u32 t0_ = ctl[C_T0]; \
    while ((int)((u32)rtime() - t0_) < (((NT - BURST) * PACE) >> 8) + POLL_GATE) __builtin_amdgcn_s_sleep(2); } } while (0)

#define HELP_GATHER(w) do { \
  if (NGW == 4) { \
    if (ok) ok = lds_wait_ge<0>(ctl + C_PUB, 1u, ctl + C_ABORT); \
    pause(DELAY1); \
    POLL_WAIT(); \
    float M_, L_, o_[4]; \
    ok = gather_part(rs, tag, (int)blockIdx.x >> 3, (int)blockIdx.x & 7, (w) * NPW, lane, ok, ctl + C_ABORT, M_, L_, o_); \
    LAS float* wp_ = LDSP(float, L_WP) + (w) * 40; \
    if (lane < 8) *(LAS f32x4*)(wp_ + lane * 4) = f32x4{o_[0], o_[1], o_[2], o_[3]}; \
    if (lane == 8) { wp_[32] = M_; wp_[33] = L_; } \
    LGKM0(); \
    if (lane == 0) __hip_atomic_fetch_add((LAS u32*)(ctl + C_GCNT), 1u, __ATOMIC_RELAXED, __HIP_MEMORY_SCOPE_WORKGROUP); \
  } \
  if (blockIdx.x < 56) {                         \
    if (ok) ok = lds_wait_ge<2>(ctl + C_YPUB, 1u, ctl + C_ABORT); \
    pause(DELAY2); \
    float s0_, s1_; \
    ok = final_share(rs, tag, (int)blockIdx.x, (w), lane, ok, ctl + C_ABORT, s0_, s1_); \
    if (lane < 8) *LDSP(u32x2, L_FP + (w) * 64 + lane * 8) = u32x2{__float_as_uint(s0_), __float_as_uint(s1_)}; \
    LGKM0(); \
    if (lane == 0) __hip_atomic_fetch_add((LAS u32*)(ctl + C_FCNT), 1u, __ATOMIC_RELAXED, __HIP_MEMORY_SCOPE_WORKGROUP); \
  } } while (0)

__global__ void __launch_bounds__(NTHREADS) fused_decode_kernel(Params P) {
  extern __shared__ __attribute__((aligned(16))) unsigned char lds[];
  const int tid = threadIdx.x, lane = tid & 63;
  const int wave = __builtin_amdgcn_readfirstlane(tid >> 6);
  const int wg = blockIdx.x, grp = wg & (NGRP - 1), rank = wg / NGRP;
  volatile LAS u32* ctl = LDSP(volatile u32, L_CTL);

  if (wave == 0) {
    if (lane < 16) ctl[lane] = 0u;
    LGKM0();
  }
  __builtin_amdgcn_s_barrier();

  bool ok = true;

  if (wave == 0) {
    const unsigned koff = (lane & 15) * 512 + (lane >> 4) * 16;
    const unsigned voff = (lane >> 4) * 2048 + (lane & 15) * 16;
    STAMP(0);
    const u32 tstart = (u32)rtime();
    ctl[C_T0] = tstart;
    u32 published = 0, freed[NCONS] = {0u, 0u};
    u32 dst = L_RING;
    int due = -BURST * PACE;
    int n = 0;
#pragma unroll 1
    for (int p = 0; p < NPH; ++p) {
      const size_t hoff = (size_t)ph_kv(p, grp) * SK * 512;
      const unsigned char* kb = (const unsigned char*)(ph_isA(p) ? P.kA : P.kB) + hoff + koff;
      const unsigned char* vb = (const unsigned char*)(ph_isA(p) ? P.vA : P.vB) + hoff + voff;
#pragma unroll 1
      for (int rd = 0; rd < TPP / TCH; ++rd) {
        const int rr = (rank + ROT * (p * (TPP / TCH) + rd)) & (WPG - 1);
        const size_t coff = (size_t)(rd * (TCH * WPG) + rr * TCH) * TILE_B;
        const unsigned char* kp = kb + coff;
        const unsigned char* vp = vb + coff;
#pragma unroll 1
        for (int i = 0; i < TCH; i += 2) {
#pragma unroll
          for (int c = 0; c < NCONS; ++c) {
            if (n >= NSLOT) {
              constexpr int cf0 = (NSLOT & 1);
              const int cf = c ^ cf0;
              const u32 need = n - NSLOT + 1;
              if (freed[cf] < need) {
                freed[cf] = ctl[C_DONE + cf];
                if (freed[cf] < need && ok) {
                  asm volatile("s_waitcnt vmcnt(0)" ::: "memory");
                  published = n; ctl[C_FULL] = published;
                  ok = lds_wait_ge<1>(ctl + C_DONE + cf, need, ctl + C_ABORT);
                  freed[cf] = need;
                }
                asm volatile("" ::: "memory");
              }
            }
            if (PACE && c == 0) {
              while ((int)((u32)rtime() - tstart) < (due >> 8)) __builtin_amdgcn_s_sleep(1);
              due += 2 * PACE;
            }
            issue_tile<INFL>(kp, vp, lds, dst, n, published, ctl);
            kp += TILE_B; vp += TILE_B; ++n;
            dst = (dst + SLOT_B == L_RING + NSLOT * SLOT_B) ? L_RING : dst + SLOT_B;
          }
        }
      }
    }
    STAMP(2);
    if (INFL > 48) { asm volatile("s_waitcnt vmcnt(48)" ::: "memory"); if (published < NT - 3) { published = NT - 3; ctl[C_FULL] = NT - 3; } }
    if (INFL > 32) { asm volatile("s_waitcnt vmcnt(32)" ::: "memory"); if (published < NT - 2) { published = NT - 2; ctl[C_FULL] = NT - 2; } }
    if (INFL > 16) { asm volatile("s_waitcnt vmcnt(16)" ::: "memory"); if (published < NT - 1) { published = NT - 1; ctl[C_FULL] = NT - 1; } }
    asm volatile("s_waitcnt vmcnt(0)" ::: "memory");
    ctl[C_FULL] = NT;
    STAMP(1);
    {
      const u32 tag = ctl[C_TAG];
      const __amdgpu_buffer_rsrc_t rs = __builtin_amdgcn_make_buffer_rsrc(P.ws + W_REG + (size_t)__builtin_amdgcn_readfirstlane(ctl[C_REG]) * REG_B, 0, REG_B, 0x00020000);
      HELP_GATHER(0);
    }

  } else if (wave <= NCONS) {
    const int c = wave - 1;
    const int cc = lane & 15, g = lane >> 4;
    u32 mask[4];
#pragma unroll
    for (int q = 0; q < 4; ++q) mask[q] = (q == ((cc & 7) >> 1)) ? ((cc & 1) ? 0xFFFF0000u : 0x0000FFFFu) : 0u;
    Tile pre[PB > 0 ? PB : 1];
#pragma unroll
    for (int k = 0; k < PB; ++k) {
      const int n = c + NCONS * k;
      if (ok) ok = lds_wait_ge<1>(ctl + C_FULL, (u32)(n + 1), ctl + C_ABORT);
      asm volatile("" ::: "memory");
      tile_read(pre[k], (const LAS unsigned char*)lds + L_RING + (n % NSLOT) * SLOT_B + lane * 16);
      LGKM0();
      ctl[C_DONE + c] = (u32)(n + 1);
    }
    int n = c + NCONS * PB;
#pragma unroll 1
    for (int p = 0; p < NPH; ++p) {
      const bool isA = ph_isA(p);
      const int s0i = ph_slot(p);
      if (ok) ok = lds_wait_ge<1>(ctl + C_QRDY, (u32)(s0i + (isA ? 2 : 1)), ctl + C_ABORT);
      asm volatile("" ::: "memory");
      if (p == 0 && c == 0) STAMP(3);
      const int qslot = s0i + (isA ? (cc >> 3) : 0);
      u32x4 qf[8];
#pragma unroll
      for (int s = 0; s < 8; ++s) qf[s] = *LDSP(const u32x4, L_Q + qslot * 512 + s * 64 + g * 16);
      Acc a;
      a.m = -INFINITY; a.l = 0.f; a.o0 = f32x4{0.f, 0.f, 0.f, 0.f}; a.o1 = f32x4{0.f, 0.f, 0.f, 0.f};
      if (p == 0) {
#pragma unroll
        for (int k = 0; k < PB; ++k) tile_math(pre[k], qf, mask, a);
      }
      u32 tag = 0, reg = 0;
      if (p == NPH - 1) { tag = ctl[C_TAG]; reg = __builtin_amdgcn_readfirstlane(ctl[C_REG]); }
      const int nend = (p + 1) * TPP;
#pragma unroll 1
      for (; n < nend; n += NCONS) {
        if (ok) ok = lds_wait_ge<CONS_SLEEP>(ctl + C_FULL, (u32)(n + 1), ctl + C_ABORT);
        asm volatile("" ::: "memory");
        Tile t;
        tile_read(t, (const LAS unsigned char*)lds + L_RING + (n % NSLOT) * SLOT_B + lane * 16);
        LGKM0();
        ctl[C_DONE + c] = (u32)(n + 1);
        tile_math(t, qf, mask, a);
      }
      const float lt = xrow16_sum(a.l);
      const int hd = cc >> 3;
      if (((NPH - 1 - p) & 1) == c) {
        LAS f32x4* r = LDSP(f32x4, L_MRG + c * MRG_B);
        r[lane] = a.o0; r[64 + lane] = a.o1;
        *LDSP(u32x2, L_MRG + c * MRG_B + 2048 + lane * 8) = u32x2{__float_as_uint(a.m), __float_as_uint(lt)};
        LGKM0();
        ctl[C_DUMP + c] = (u32)(p + 1);
      } else {
        if (ok) ok = lds_wait_ge<0>(ctl + C_DUMP + (1 - c), (u32)(p + 1), ctl + C_ABORT);
        asm volatile("" ::: "memory");
        const LAS f32x4* r = LDSP(const f32x4, L_MRG + (1 - c) * MRG_B);
        const f32x4 b0 = r[lane], b1 = r[64 + lane];
        const u32x2 ml2 = *LDSP(const u32x2, L_MRG + (1 - c) * MRG_B + 2048 + lane * 8);
        const float m2 = __uint_as_float(ml2[0]), M = fmaxf(a.m, m2);
        const float w1 = __builtin_amdgcn_exp2f(a.m - M), w2 = __builtin_amdgcn_exp2f(m2 - M);
        const f32x4 x0 = w1 * a.o0 + w2 * b0, x1 = w1 * a.o1 + w2 * b1;
        const float L = fmaf(w1, lt, w2 * __uint_as_float(ml2[1]));
        const u32x2 r0 = {pack_bf16(x0[0], x0[1]), pack_bf16(x0[2], x0[3])}, r1 = {pack_bf16(x1[0], x1[1]), pack_bf16(x1[2], x1[3])};
        if (p < NPH - 1) {
          if (isA || hd == 0) {
            const int qo = L_Q + (s0i + hd) * 512 + g * 64 + (cc & 7) * 8;
            *LDSP(u32x2, qo) = r0;
            *LDSP(u32x2, qo + 256) = r1;
            if (g == 0 && (cc & 7) == 0) *LDSP(u32x2, L_ML + (s0i + hd) * 8) = u32x2{__float_as_uint(M), __float_as_uint(L)};
          }
          LGKM0();
          ctl[C_MERGED] = (u32)(p + 1);
        } else {
          const __amdgpu_buffer_rsrc_t rs = __builtin_amdgcn_make_buffer_rsrc(P.ws + W_REG + (size_t)reg * REG_B, 0, REG_B, 0x00020000);
          const int qh = slot_qh(NQS - 1, grp);
          const u32 o = R_PART + ((qh * 8 + g) * WPG + rank) * 128 + (cc & 7) * 16;
          ST_SC1((u32x4{r0[0], tag, r0[1], tag}), rs, hd == 0 ? o : OOB);
          ST_SC1((u32x4{r1[0], tag, r1[1], tag}), rs, hd == 0 ? o + 4 * WPG * 128 : OOB);
          ST_SC1((u32x4{__float_as_uint(M), tag, __float_as_uint(L), tag}), rs, lane == 0 ? R_ML + (qh * WPG + rank) * 16 : OOB);
          ctl[C_PUB] = 1u;
          STAMP(10);
        }
      }
    }
    if (c == 0) STAMP(4);
    {
      const u32 tag = ctl[C_TAG];
      const __amdgpu_buffer_rsrc_t rs = __builtin_amdgcn_make_buffer_rsrc(P.ws + W_REG + (size_t)__builtin_amdgcn_readfirstlane(ctl[C_REG]) * REG_B, 0, REG_B, 0x00020000);
      HELP_GATHER(wave);
    }
  } else {
    const u32 tsvc = (u32)rtime();
    GAS u64* epoch_p = (GAS u64*)(P.ws + W_EPOCH);
    const u64 e0 = __hip_atomic_load(epoch_p, RLX_AGENT);
    const int prob = wg >> 7, w7 = wg & 127;
    const int sl = ((w7 >> 4) << 4) + ((w7 & 7) << 1) + ((w7 >> 3) & 1);
    const int a = lane >> 2, ch = lane & 3;
    const unsigned char* wq = (const unsigned char*)(prob ? P.wqB : P.wqA) + (size_t)(28 * a) * 8192 + sl * 64 + ch * 16;
    const unsigned char* xp = (const unsigned char*)(prob ? P.xB : P.xA) + a * 56;
    u32x4 wv[28];
    u32x2 xv[7];
#pragma unroll
    for (int i = 0; i < 7; ++i) xv[i] = __builtin_nontemporal_load((const GAS u32x2*)(xp + i * 8));
#pragma unroll
    for (int i = 0; i < 28; ++i) wv[i] = ld_nt(wq + (size_t)i * 8192);
    STAMP(5);
    const u32 tag = (u32)(mix64(e0) >> 32);
    const u32 reg = __builtin_amdgcn_readfirstlane((u32)e0 & (NREG - 1));
    ctl[C_TAG] = tag; ctl[C_REG] = reg;
    const __amdgpu_buffer_rsrc_t rs = __builtin_amdgcn_make_buffer_rsrc(P.ws + W_REG + (size_t)reg * REG_B, 0, REG_B, 0x00020000);
    float acc[8];
#pragma unroll
    for (int k = 0; k < 8; ++k) acc[k] = 0.f;
#pragma unroll
    for (int i = 0; i < 14; ++i) {
      const u32 xx = xv[i >> 1][i & 1];
#pragma unroll
      for (int d = 0; d < 4; ++d) {
        const u32 w0 = wv[2 * i][d], w1 = wv[2 * i + 1][d];
        acc[2 * d] = dot2(__builtin_amdgcn_perm(w1, w0, 0x05040100u), xx, acc[2 * d]);
        acc[2 * d + 1] = dot2(__builtin_amdgcn_perm(w1, w0, 0x07060302u), xx, acc[2 * d + 1]);
      }
    }
#pragma unroll
    for (int k = 0; k < 8; ++k) {
      float v = acc[k];
      v += dpp<D_ROR4>(v); v += dpp<D_ROR8>(v);
      acc[k] = xrow16_sum(v);
    }
    {
      u32 pk = pack_bf16(acc[0], acc[1]);
#pragma unroll
      for (int k = 1; k < 4; ++k) pk = (a == k) ? pack_bf16(acc[2 * k], acc[2 * k + 1]) : pk;
      if (a < 4) __hip_atomic_store((GAS u64*)(P.ws + W_REG + (size_t)reg * REG_B + R_QG) + prob * 2048 + sl * 16 + ch * 4 + a, ((u64)tag << 32) | pk, RLX_AGENT);
    }
    STAMP(6);
    {
      u32x4 v;
      const u64 t0 = rtime();
      for (u32 sp = 1; ok; ++sp) {
        v = LD_SC1(rs, R_QG + slot_qh(0, grp) * 1024 + lane * 16);
        if (__all(tagged(v, tag))) break;
        __builtin_amdgcn_s_sleep(4);
        if (poll_expired(sp, t0, ctl + C_ABORT)) ok = false;
      }
      *LDSP(u32x2, L_Q + lane * 8) = u32x2{v[0], v[2]};
      LGKM0();
      ctl[C_QRDY] = 1u;
      STAMP(7);
    }
    {
      constexpr int QB = (NQS - 1) < 16 ? (NQS - 1) : 16;
#pragma unroll 1
      for (int q0 = 1; q0 < NQS; q0 += QB) {
        u32x4 v[QB];
        const u64 t0 = rtime();
        for (u32 sp = 1; ok; ++sp) {
          bool good = true;
#pragma unroll
          for (int i = 0; i < QB; ++i) {
            const int qs = (q0 + i < NQS) ? q0 + i : NQS - 1;
            v[i] = LD_SC1(rs, R_QG + slot_qh(qs, grp) * 1024 + lane * 16);
          }
#pragma unroll
          for (int i = 0; i < QB; ++i) good &= tagged(v[i], tag);
          if (__all(good)) break;
          __builtin_amdgcn_s_sleep(8);
          if (poll_expired(sp, t0, ctl + C_ABORT)) ok = false;
        }
#pragma unroll
        for (int i = 0; i < QB; ++i)
          if (q0 + i < NQS) *LDSP(u32x2, L_Q + (q0 + i) * 512 + lane * 8) = u32x2{v[i][0], v[i][2]};
        LGKM0();
        ctl[C_QRDY] = (u32)((q0 + QB < NQS) ? q0 + QB : NQS);
      }
    }
    STAMP(8);
    const int uq = wg >> 3, us = wg & 7;
    u32 wo[32][4];
    {
      const unsigned char* wp = (const unsigned char*)(uq >= 16 ? P.woB : P.woA) + (size_t)((uq & 15) * 256 + us * 32) * 896 + lane * 4;
#pragma unroll
      for (int k = 0; k < 32; ++k)
#pragma unroll
        for (int i = 0; i < 4; ++i)
          wo[k][i] = __builtin_nontemporal_load((const GAS u32*)(wp + (8 * (k & 3) + (k >> 2)) * 896 + ((i < 3 || lane < 32) ? i * 256 : 0)));
#pragma unroll
      for (int kp = 0; kp < 16; ++kp)
#pragma unroll
        for (int i = 0; i < 4; ++i) {
          const u32 w0 = wo[2 * kp][i], w1 = wo[2 * kp + 1][i];
          wo[2 * kp][i] = __builtin_amdgcn_perm(w1, w0, 0x05040100u);
          wo[2 * kp + 1][i] = __builtin_amdgcn_perm(w1, w0, 0x07060302u);
          asm volatile("" : "+v"(wo[2 * kp][i]), "+v"(wo[2 * kp + 1][i]));
        }
    }
    if (ok) ok = lds_wait_ge<16>(ctl + C_MERGED, (u32)(NPH - 1), ctl + C_ABORT);
    while (ctl[C_FULL] < (u32)(NT - OLD_LEAD) && (!PACE || (int)((u32)rtime() - tsvc) < (((NT - BURST) * PACE) >> 8) + OLD_GATE) && ctl[C_ABORT] == 0u) __builtin_amdgcn_s_sleep(4);
    asm volatile("" ::: "memory");
#pragma unroll 4
    for (int qs = 0; qs < NQS - 1; ++qs) {
      const u32x2 v = *LDSP(const u32x2, L_Q + qs * 512 + lane * 8);
      ST_SC1((u32x4{v[0], tag, v[1], tag}), rs, R_PART + ((slot_qh(qs, grp) * 8 + (lane >> 3)) * WPG + rank) * 128 + (lane & 7) * 16);
    }
    {
      const int qs = lane < NQS - 1 ? lane : 0;
      const u32x2 v = *LDSP(const u32x2, L_ML + qs * 8);
      ST_SC1((u32x4{v[0], tag, v[1], tag}), rs, lane < NQS - 1 ? R_ML + (slot_qh(qs, grp) * WPG + rank) * 16 : OOB);
    }
    STAMP(9);
    if (ok) ok = lds_wait_ge<0>(ctl + C_PUB, 1u, ctl + C_ABORT);
    pause(DELAY1);
    POLL_WAIT();
    float M, L, o[4];
    ok = gather_part(rs, tag, uq, us, NGW == 4 ? 3 * NPW : 0, lane, ok, ctl + C_ABORT, M, L, o);
    if (NGW == 4) {
      if (ok) ok = lds_wait_ge<0>(ctl + C_GCNT, 3u, ctl + C_ABORT);
      asm volatile("" ::: "memory");
      const LAS float* wp = LDSP(const float, L_WP);
      float Mt = M;
#pragma unroll
      for (int w = 0; w < 3; ++w) Mt = fmaxf(Mt, wp[w * 40 + 32]);
      const float f3 = __builtin_amdgcn_exp2f(M - Mt);
      L *= f3;
#pragma unroll
      for (int k = 0; k < 4; ++k) o[k] *= f3;
#pragma unroll
      for (int w = 0; w < 3; ++w) {
        const float f = __builtin_amdgcn_exp2f(wp[w * 40 + 32] - Mt);
        const f32x4 ow = *(const LAS f32x4*)(wp + w * 40 + (lane & 7) * 4);
        L = fmaf(f, wp[w * 40 + 33], L);
#pragma unroll
        for (int k = 0; k < 4; ++k) o[k] = fmaf(f, ow[k], o[k]);
      }
    }
    STAMP(11);
    {
      const float inv = 1.f / L;
      const u32 op0 = pack_bf16(o[0] * inv, o[1] * inv), op1 = pack_bf16(o[2] * inv, o[3] * inv);
      float y[4][2];
#pragma unroll
      for (int i = 0; i < 4; ++i) { y[i][0] = 0.f; y[i][1] = 0.f; }
#pragma unroll
      for (int k = 0; k < 16; ++k) {
        const u32 ov = __builtin_amdgcn_readlane((k & 1) ? op1 : op0, k >> 1);
#pragma unroll
        for (int i = 0; i < 4; ++i) {
          y[i][0] = dot2(wo[2 * k][i], ov, y[i][0]);
          y[i][1] = dot2(wo[2 * k + 1][i], ov, y[i][1]);
        }
      }
      const u32 yo = R_Y + (((uq >> 4) * 28 + (lane >> 3)) * 128 + (wg & 127)) * 128 + (lane & 7) * 16;
#pragma unroll
      for (int i = 0; i < 4; ++i)
        ST_SC1((u32x4{__float_as_uint(y[i][0]), tag, __float_as_uint(y[i][1]), tag}), rs, (i < 3 || lane < 32) ? yo + i * 8 * 128 * 128 : OOB);
    }
    STAMP(12);
    if (wg < 56) {
      ctl[C_YPUB] = 1u;
      pause(DELAY2);
      float s0, s1;
      ok = final_share(rs, tag, wg, FW - 1, lane, ok, ctl + C_ABORT, s0, s1);
      if (FW == 4) {
        if (ok) ok = lds_wait_ge<0>(ctl + C_FCNT, 3u, ctl + C_ABORT);
        asm volatile("" ::: "memory");
        const u32x2 v0 = *LDSP(const u32x2, L_FP + (lane & 7) * 8), v1 = *LDSP(const u32x2, L_FP + 64 + (lane & 7) * 8), v2 = *LDSP(const u32x2, L_FP + 128 + (lane & 7) * 8);
        s0 = ((__uint_as_float(v0[0]) + __uint_as_float(v1[0])) + __uint_as_float(v2[0])) + s0;
        s1 = ((__uint_as_float(v0[1]) + __uint_as_float(v1[1])) + __uint_as_float(v2[1])) + s1;
      }
      if (lane < 8) __hip_atomic_store((GAS u32*)(P.out + (wg / 28) * DM + (wg % 28) * 16 + lane * 2), pack_bf16(s0, s1), RLX_AGENT);
      if (wg == 28 && lane == 0) __hip_atomic_store(epoch_p, e0 + 1, RLX_AGENT);
      STAMP(13);
    }
  }
}

extern "C" void kernel_launch(void* const* d_in, const int* in_sizes, int n_in, void* d_out, int out_size, void* d_ws, size_t ws_size,
                              hipStream_t stream) {
  (void)in_sizes; (void)n_in; (void)out_size;
  Params p;
  p.xA = (const unsigned short*)d_in[0]; p.kA = (const unsigned short*)d_in[1]; p.vA = (const unsigned short*)d_in[2];
  p.wqA = (const unsigned short*)d_in[3]; p.woA = (const unsigned short*)d_in[4];
  p.xB = (const unsigned short*)d_in[5]; p.kB = (const unsigned short*)d_in[6]; p.vB = (const unsigned short*)d_in[7];
  p.wqB = (const unsigned short*)d_in[8]; p.woB = (const unsigned short*)d_in[9];
  p.out = (unsigned short*)d_out;
  p.ws = (unsigned char*)d_ws + ((ws_size - WS_BYTES) & ~(size_t)4095);
  (void)hipFuncSetAttribute((const void*)fused_decode_kernel, hipFuncAttributeMaxDynamicSharedMemorySize, L_END);
  hipLaunchKernelGGL(fused_decode_kernel, dim3(NWG), dim3(NTHREADS), L_END, stream, p);
}
```

```cpp
#include <hip/hip_runtime.h>
#include <hip/hip_bf16.h>
#include <stdint.h>

#define GAS __attribute__((address_space(1)))
#define LAS __attribute__((address_space(3)))
typedef unsigned int u32;
typedef unsigned long long u64;
typedef u32 u32x4 __attribute__((ext_vector_type(4)));
typedef u32 u32x2 __attribute__((ext_vector_type(2)));
typedef float f32x4 __attribute__((ext_vector_type(4)));
typedef __bf16 bf16x8 __attribute__((ext_vector_type(8)));
typedef __bf16 bf16x2 __attribute__((ext_vector_type(2)));
#define RLX_AGENT __ATOMIC_RELAXED, __HIP_MEMORY_SCOPE_AGENT

constexpr int DM = 448;
constexpr int SK = 32768;
constexpr int NWG = 256;
#define NGRP 2
constexpr int WPG = NWG / NGRP;
constexpr int NA = 8 / NGRP, NB = 16 / NGRP;
constexpr int NPH = NA + NB;
constexpr int NQS = 2 * NA + NB;
constexpr int TILE_B = 8192;
constexpr int TPH = SK / 16;
constexpr int TPP = TPH / WPG;
#define TCH (2 * NGRP)
constexpr int NT = NPH * TPP;
constexpr int NCONS = 2;
#define NSLOT (NQS > 16 ? 8 : 9)
#define PB 4
constexpr int SLOT_B = 2 * TILE_B;
constexpr int NTHREADS = 64 * (NCONS + 2);
constexpr float SCL = 0.0625f * 1.4426950408889634f;
static_assert(TPP % TCH == 0 && NT == 192 && TPP >= 2 * PB, "tiling");

__host__ __device__ constexpr bool ph_isA(int p) { return p >= NB / 2 && p < NB / 2 + NA; }
__host__ __device__ constexpr int ph_kv(int p, int g) { return ph_isA(p) ? g * NA + (p - NB / 2) : g * NB + (p < NB / 2 ? p : p - NA); }
__host__ __device__ constexpr int ph_slot(int p) { return p <= NB / 2 ? p : (p < NB / 2 + NA ? NB / 2 + 2 * (p - NB / 2) : p + NA); }
__host__ __device__ constexpr int slot_qh(int s, int g) { return s < NB / 2 ? 16 + g * NB + s : (s < NB / 2 + 2 * NA ? g * 2 * NA + (s - NB / 2) : 16 + g * NB + (s - 2 * NA)); }
static_assert(!ph_isA(NPH - 1), "the last phase carries one query head");

constexpr int L_RING = 0;
constexpr int L_Q = NSLOT * SLOT_B;
constexpr int L_ML = L_Q + NQS * 512;
constexpr int MRG_B = 2048 + 512;
constexpr int L_MRG = L_ML + NQS * 8;
constexpr int L_CTL = L_MRG + NCONS * MRG_B;
constexpr int L_END = L_CTL + 64;
static_assert(L_END <= 163840, "LDS budget");
constexpr int C_FULL = 0, C_DONE = 1  , C_QRDY = 4  , C_DUMP = 5  , C_ABORT = 7, C_TAG = 8, C_REG = 9,
              C_MERGED = 10, C_PUB = 11, C_GCNT = 12, C_T0 = 13, C_YPUB = 14, C_FCNT = 15;
constexpr int L_WP = L_RING;
constexpr int L_FP = L_RING + 1024;

constexpr int NREG = 16;
constexpr u32 R_QG = 0;
constexpr u32 R_PART = R_QG + 4096 * 8;
constexpr u32 R_ML = R_PART + 32 * 8 * WPG * 128;
constexpr u32 R_Y = R_ML + 32 * WPG * 16;
constexpr u32 REG_B = (R_Y + 2 * 28 * 128 * 128 + 4095) / 4096 * 4096;
constexpr size_t W_EPOCH = 0;
constexpr size_t W_REG = 4096;
constexpr size_t WS_BYTES = W_REG + (size_t)NREG * REG_B;
constexpr u32 OOB = 0x7FFFFFF0u;

struct Params {
  const unsigned short *xA, *kA, *vA, *wqA, *woA, *xB, *kB, *vB, *wqB, *woB;
  unsigned short* out;
  unsigned char* ws;
};

template <int CTRL> __device__ __forceinline__ float dpp(float x) {
  return __builtin_bit_cast(float, __builtin_amdgcn_mov_dpp(__builtin_bit_cast(int, x), CTRL, 0xf, 0xf, true));
}
constexpr int D_XOR1 = 0xB1, D_XOR2 = 0x4E, D_ROR4 = 0x124, D_ROR8 = 0x128;
__device__ __forceinline__ float xrow16_max(float x) {
  auto s = __builtin_amdgcn_permlane16_swap(__float_as_uint(x), __float_as_uint(x), false, false);
  x = fmaxf(__uint_as_float(s[0]), __uint_as_float(s[1]));
  auto t = __builtin_amdgcn_permlane32_swap(__float_as_uint(x), __float_as_uint(x), false, false);
  return fmaxf(__uint_as_float(t[0]), __uint_as_float(t[1]));
}
__device__ __forceinline__ float xrow16_sum(float x) {
  auto s = __builtin_amdgcn_permlane16_swap(__float_as_uint(x), __float_as_uint(x), false, false);
  x = __uint_as_float(s[0]) + __uint_as_float(s[1]);
  auto t = __builtin_amdgcn_permlane32_swap(__float_as_uint(x), __float_as_uint(x), false, false);
  return __uint_as_float(t[0]) + __uint_as_float(t[1]);
}
__device__ __forceinline__ float oct_max(float x) {
  return xrow16_max(fmaxf(x, dpp<D_ROR8>(x)));
}
__device__ __forceinline__ float oct_sum(float x) {
  return xrow16_sum(x + dpp<D_ROR8>(x));
}
__device__ __forceinline__ u32 pack_bf16(float lo, float hi) {
  bf16x2 v = {(__bf16)lo, (__bf16)hi};
  return __builtin_bit_cast(u32, v);
}
__device__ __forceinline__ float bf_lo(u32 v) { return __uint_as_float(v << 16); }
__device__ __forceinline__ float bf_hi(u32 v) { return __uint_as_float(v & 0xFFFF0000u); }
__device__ __forceinline__ float round_bf16(float x) { return bf_lo(pack_bf16(x, x)); }
__device__ __forceinline__ float dot2(u32 a, u32 b, float acc) {
  return __builtin_amdgcn_fdot2_f32_bf16(__builtin_bit_cast(bf16x2, a), __builtin_bit_cast(bf16x2, b), acc, false);
}
__device__ __forceinline__ f32x4 mfma16(u32x4 a, u32x4 b, f32x4 c) {
  return __builtin_amdgcn_mfma_f32_16x16x32_bf16(__builtin_bit_cast(bf16x8, a), __builtin_bit_cast(bf16x8, b), c, 0, 0, 0);
}
__device__ __forceinline__ u64 mix64(u64 z) {
  z += 0x9E3779B97F4A7C15ull;
  z = (z ^ (z >> 30)) * 0xBF58476D1CE4E5B9ull;
  z = (z ^ (z >> 27)) * 0x94D049BB133111EBull;
  return z ^ (z >> 31);
}
__device__ __forceinline__ u64 rtime() { return __builtin_amdgcn_s_memrealtime(); }
__device__ __forceinline__ void pause(long long ticks) {
  const u64 t0 = rtime();
  while ((long long)(rtime() - t0) < ticks) __builtin_amdgcn_s_sleep(1);
}
constexpr long long SPIN_CAP = 20000000;
__device__ __forceinline__ u32x4 ld_nt(const void* p) { return __builtin_nontemporal_load((const GAS u32x4*)p); }
__device__ __forceinline__ bool tagged(u32x4 v, u32 tag) { return (v[1] == tag) & (v[3] == tag); }

#define LDSP(T, off) ((LAS T*)((LAS unsigned char*)lds + (off)))
#define LGKM0() asm volatile("s_waitcnt lgkmcnt(0)" ::: "memory")
#define ST_SC1(v, rs, off) __builtin_amdgcn_raw_buffer_store_b128((v), (rs), (off), 0, 16)
#define LD_SC1(rs, off) __builtin_amdgcn_raw_buffer_load_b128((rs), (off), 0, 16)

template <int SLEEP>
__device__ __forceinline__ bool lds_wait_ge(volatile LAS u32* w, u32 need, volatile LAS u32* abortw) {
  if (*w >= need) return true;
  const u64 t0 = rtime();
  for (u32 sp = 1;; ++sp) {
    if (*w >= need) return true;
    if (SLEEP) __builtin_amdgcn_s_sleep(SLEEP);
    if ((sp & 255u) == 0u) {
      if (*abortw != 0u) return false;
      if ((long long)(rtime() - t0) > SPIN_CAP) { *abortw = 1u; return false; }
    }
  }
}
__device__ __forceinline__ bool poll_expired(u32 sp, u64 t0, volatile LAS u32* abortw) {
  if ((sp & 63u) == 0u && (long long)(rtime() - t0) > SPIN_CAP) { *abortw = 1u; return true; }
  return false;
}

#define KV_AUX 2
#define INFL 48
#define ROT 1
#define PACE 14533
#define BURST 4
#define OLD_LEAD 2
#define OLD_GATE 100
#define POLL_GATE 200
#define CONS_SLEEP 1
#define DELAY1 10
#define REPOLL 0
#define DELAY2 30
#define WAITVM(n) asm volatile("s_waitcnt vmcnt(%0)" :: "i"(n) : "memory")
#define PIECE(src, goff, loff) __builtin_amdgcn_global_load_lds((const GAS u32*)(src), (LAS u32*)((LAS unsigned char*)lds + dst + (loff) - (goff)), 16, (goff), KV_AUX)
template <int WIN>
__device__ __forceinline__ void issue_tile(const unsigned char* kp, const unsigned char* vp, unsigned char* lds, u32 dst, int n, u32& published, volatile LAS u32* ctl) {
#define AFTER(h) do { WAITVM(WIN); if ((WIN - 4 * ((h) + 1)) % 16 == 0) {         \
    const int landed = n - (WIN - 4 * ((h) + 1)) / 16; if (landed > (int)published) { published = landed; ctl[C_FULL] = published; } } } while (0)
  PIECE(kp, 0, 0); PIECE(kp, 64, 1024); PIECE(kp, 128, 2048); PIECE(kp, 192, 3072);
  AFTER(0);
  PIECE(kp, 256, 4096); PIECE(kp, 320, 5120); PIECE(kp, 384, 6144); PIECE(kp, 448, 7168);
  AFTER(1);
  PIECE(vp, 0, 8192); PIECE(vp, 256, 9216); PIECE(vp, 512, 10240); PIECE(vp, 768, 11264);
  AFTER(2);
  PIECE(vp, 1024, 12288); PIECE(vp, 1280, 13312); PIECE(vp, 1536, 14336); PIECE(vp, 1792, 15360);
  AFTER(3);
#undef AFTER
}

struct Tile { u32x4 k[8], v[8]; };
struct Acc { float m, l; f32x4 o0, o1; };

__device__ __forceinline__ void tile_read(Tile& t, const LAS unsigned char* sb) {
#pragma unroll
  for (int j = 0; j < 8; ++j) t.k[j] = *(const LAS u32x4*)(sb + j * 1024);
#pragma unroll
  for (int j = 0; j < 8; ++j) t.v[j] = *(const LAS u32x4*)(sb + 8192 + j * 1024);
}
__device__ __forceinline__ void tile_math(const Tile& t, const u32x4 (&qf)[8], const u32 (&mask)[4], Acc& a) {
  f32x4 s0 = {0.f, 0.f, 0.f, 0.f}, s1 = {0.f, 0.f, 0.f, 0.f};
#pragma unroll
  for (int j = 0; j < 8; j += 2) { s0 = mfma16(t.k[j], qf[j], s0); s1 = mfma16(t.k[j + 1], qf[j + 1], s1); }
  float s[4];
#pragma unroll
  for (int r = 0; r < 4; ++r) s[r] = round_bf16(s0[r] + s1[r]);
  float mx = fmaxf(fmaxf(s[0], s[1]), fmaxf(s[2], s[3]));
  mx = xrow16_max(mx);
  const float mn = fmaxf(a.m, mx * SCL);
  const float alpha = __builtin_amdgcn_exp2f(a.m - mn);
  a.m = mn;
  float pr[4];
#pragma unroll
  for (int r = 0; r < 4; ++r) pr[r] = __builtin_amdgcn_exp2f(fmaf(s[r], SCL, -mn));
  a.l = fmaf(a.l, alpha, (pr[0] + pr[1]) + (pr[2] + pr[3]));
  a.o0 *= alpha; a.o1 *= alpha;
#pragma unroll
  for (int r = 0; r < 4; ++r) {
    const u32 pk = pack_bf16(pr[r], pr[r]);
    const u32x4 b = {pk & mask[0], pk & mask[1], pk & mask[2], pk & mask[3]};
    a.o0 = mfma16(t.v[2 * r], b, a.o0);
    a.o1 = mfma16(t.v[2 * r + 1], b, a.o1);
  }
}

template <int N, class F>
__device__ __forceinline__ bool gather_granules(const __amdgpu_buffer_rsrc_t rs, u32 tag, u32x4 (&v)[N], F off, bool ok, volatile LAS u32* abortw) {
#pragma unroll
  for (int i = 0; i < N; ++i) v[i] = LD_SC1(rs, off(i));
  const u64 t0 = rtime();
  for (u32 sp = 1; ok; ++sp) {
    bool good = true;
#pragma unroll
    for (int i = 0; i < N; ++i) good &= tagged(v[i], tag);
    if (__all(good)) break;
    if (REPOLL) pause(REPOLL);
#pragma unroll
    for (int i = 0; i < N; ++i) {
      const bool g = tagged(v[i], tag);
      const u32x4 t = LD_SC1(rs, g ? OOB : off(i));
      v[i] = g ? v[i] : t;
    }
    if (poll_expired(sp, t0, abortw)) ok = false;
  }
  return ok;
}

#define NGW_MIN 64
constexpr int NGW = WPG >= NGW_MIN ? 4 : 1;
constexpr int NPW = WPG / NGW;
constexpr int NLD = NPW / 8;
__device__ __forceinline__ bool gather_part(const __amdgpu_buffer_rsrc_t rs, u32 tag, int uq, int us, int j0, int lane, bool ok,
                                            volatile LAS u32* abortw, float& Mo, float& Lo, float (&oo)[4]) {
  u32x4 g[2 * NLD];
  const u32 pb = R_PART + ((uq * 8 + us) * WPG + j0) * 128 + lane * 16, mb = R_ML + (uq * WPG + j0 + (lane >> 3)) * 16;
  ok = gather_granules(rs, tag, g, [&](int i) { return (i & 1) ? mb + (i >> 1) * 128 : pb + (i >> 1) * 1024; }, ok, abortw);
  float M = -INFINITY;
#pragma unroll
  for (int i = 0; i < NLD; ++i) M = fmaxf(M, __uint_as_float(g[2 * i + 1][0]));
  M = oct_max(M);
  float L = 0.f, o[4] = {0.f, 0.f, 0.f, 0.f};
#pragma unroll
  for (int i = 0; i < NLD; ++i) {
    const float w = __builtin_amdgcn_exp2f(__uint_as_float(g[2 * i + 1][0]) - M);
    L = fmaf(w, __uint_as_float(g[2 * i + 1][2]), L);
    o[0] = fmaf(w, bf_lo(g[2 * i][0]), o[0]); o[1] = fmaf(w, bf_hi(g[2 * i][0]), o[1]);
    o[2] = fmaf(w, bf_lo(g[2 * i][2]), o[2]); o[3] = fmaf(w, bf_hi(g[2 * i][2]), o[3]);
  }
  Mo = M; Lo = oct_sum(L);
#pragma unroll
  for (int k = 0; k < 4; ++k) oo[k] = oct_sum(o[k]);
  return ok;
}

constexpr int FW = 4;
__device__ __forceinline__ bool final_share(const __amdgpu_buffer_rsrc_t rs, u32 tag, int wg, int w, int lane, bool ok, volatile LAS u32* abortw,
                                            float& s0, float& s1) {
  constexpr int NL = 16 / FW;
  const u32 yb = R_Y + wg * 128 * 128 + w * NL * 1024 + lane * 16;
  u32x4 t[NL];
  ok = gather_granules(rs, tag, t, [&](int i) { return yb + i * 1024; }, ok, abortw);
  float a0 = 0.f, a1 = 0.f;
#pragma unroll
  for (int i = 0; i < NL; ++i) { a0 += __uint_as_float(t[i][0]); a1 += __uint_as_float(t[i][2]); }
  s0 = oct_sum(a0); s1 = oct_sum(a1);
  return ok;
}

#define POLL_WAIT() do { if (PACE && POLL_GATE > -100000) { const u32 t0_ = ctl[C_T0]; \
    while ((int)((u32)rtime() - t0_) < (((NT - BURST) * PACE) >> 8) + POLL_GATE) __builtin_amdgcn_s_sleep(2); } } while (0)

#define HELP_GATHER(w) do { \
  if (NGW == 4) { \
    if (ok) ok = lds_wait_ge<0>(ctl + C_PUB, 1u, ctl + C_ABORT); \
    pause(DELAY1); \
    POLL_WAIT(); \
    float M_, L_, o_[4]; \
    ok = gather_part(rs, tag, (int)blockIdx.x >> 3, (int)blockIdx.x & 7, (w) * NPW, lane, ok, ctl + C_ABORT, M_, L_, o_); \
    LAS float* wp_ = LDSP(float, L_WP) + (w) * 40; \
    if (lane < 8) *(LAS f32x4*)(wp_ + lane * 4) = f32x4{o_[0], o_[1], o_[2], o_[3]}; \
    if (lane == 8) { wp_[32] = M_; wp_[33] = L_; } \
    LGKM0(); \
    if (lane == 0) __hip_atomic_fetch_add((LAS u32*)(ctl + C_GCNT), 1u, __ATOMIC_RELAXED, __HIP_MEMORY_SCOPE_WORKGROUP); \
  } \
  if (blockIdx.x < 56) {                         \
    if (ok) ok = lds_wait_ge<2>(ctl + C_YPUB, 1u, ctl + C_ABORT); \
    pause(DELAY2); \
    float s0_, s1_; \
    ok = final_share(rs, tag, (int)blockIdx.x, (w), lane, ok, ctl + C_ABORT, s0_, s1_); \
    if (lane < 8) *LDSP(u32x2, L_FP + (w) * 64 + lane * 8) = u32x2{__float_as_uint(s0_), __float_as_uint(s1_)}; \
    LGKM0(); \
    if (lane == 0) __hip_atomic_fetch_add((LAS u32*)(ctl + C_FCNT), 1u, __ATOMIC_RELAXED, __HIP_MEMORY_SCOPE_WORKGROUP); \
  } } while (0)

__global__ void __launch_bounds__(NTHREADS) fused_decode_kernel(Params P) {
  extern __shared__ __attribute__((aligned(16))) unsigned char lds[];
  const int tid = threadIdx.x, lane = tid & 63;
  const int wave = __builtin_amdgcn_readfirstlane(tid >> 6);
  const int wg = blockIdx.x, grp = wg & (NGRP - 1), rank = wg / NGRP;
  volatile LAS u32* ctl = LDSP(volatile u32, L_CTL);

  if (wave == 0) {
    if (lane < 16) ctl[lane] = 0u;
    LGKM0();
  }
  __builtin_amdgcn_s_barrier();

  bool ok = true;

  if (wave == 0) {
    const unsigned koff = (lane & 15) * 512 + (lane >> 4) * 16;
    const unsigned voff = (lane >> 4) * 2048 + (lane & 15) * 16;
    const u32 tstart = (u32)rtime();
    ctl[C_T0] = tstart;
    u32 published = 0, freed[NCONS] = {0u, 0u};
    u32 dst = L_RING;
    int due = -BURST * PACE;
    int n = 0;
#pragma unroll 1
    for (int p = 0; p < NPH; ++p) {
      const size_t hoff = (size_t)ph_kv(p, grp) * SK * 512;
      const unsigned char* kb = (const unsigned char*)(ph_isA(p) ? P.kA : P.kB) + hoff + koff;
      const unsigned char* vb = (const unsigned char*)(ph_isA(p) ? P.vA : P.vB) + hoff + voff;
#pragma unroll 1
      for (int rd = 0; rd < TPP / TCH; ++rd) {
        const int rr = (rank + ROT * (p * (TPP / TCH) + rd)) & (WPG - 1);
        const size_t coff = (size_t)(rd * (TCH * WPG) + rr * TCH) * TILE_B;
        const unsigned char* kp = kb + coff;
        const unsigned char* vp = vb + coff;
#pragma unroll 1
        for (int i = 0; i < TCH; i += 2) {
#pragma unroll
          for (int c = 0; c < NCONS; ++c) {
            if (n >= NSLOT) {
              constexpr int cf0 = (NSLOT & 1);
              const int cf = c ^ cf0;
              const u32 need = n - NSLOT + 1;
              if (freed[cf] < need) {
                freed[cf] = ctl[C_DONE + cf];
                if (freed[cf] < need && ok) {
                  asm volatile("s_waitcnt vmcnt(0)" ::: "memory");
                  published = n; ctl[C_FULL] = published;
                  ok = lds_wait_ge<1>(ctl + C_DONE + cf, need, ctl + C_ABORT);
                  freed[cf] = need;
                }
                asm volatile("" ::: "memory");
              }
            }
            if (PACE && c == 0) {
              while ((int)((u32)rtime() - tstart) < (due >> 8)) __builtin_amdgcn_s_sleep(1);
              due += 2 * PACE;
            }
            issue_tile<INFL>(kp, vp, lds, dst, n, published, ctl);
            kp += TILE_B; vp += TILE_B; ++n;
            dst = (dst + SLOT_B == L_RING + NSLOT * SLOT_B) ? L_RING : dst + SLOT_B;
          }
        }
      }
    }
    if (INFL > 48) { asm volatile("s_waitcnt vmcnt(48)" ::: "memory"); if (published < NT - 3) { published = NT - 3; ctl[C_FULL] = NT - 3; } }
    if (INFL > 32) { asm volatile("s_waitcnt vmcnt(32)" ::: "memory"); if (published < NT - 2) { published = NT - 2; ctl[C_FULL] = NT - 2; } }
    if (INFL > 16) { asm volatile("s_waitcnt vmcnt(16)" ::: "memory"); if (published < NT - 1) { published = NT - 1; ctl[C_FULL] = NT - 1; } }
    asm volatile("s_waitcnt vmcnt(0)" ::: "memory");
    ctl[C_FULL] = NT;
    {
      const u32 tag = ctl[C_TAG];
      const __amdgpu_buffer_rsrc_t rs = __builtin_amdgcn_make_buffer_rsrc(P.ws + W_REG + (size_t)__builtin_amdgcn_readfirstlane(ctl[C_REG]) * REG_B, 0, REG_B, 0x00020000);
      HELP_GATHER(0);
    }

  } else if (wave <= NCONS) {
    const int c = wave - 1;
    const int cc = lane & 15, g = lane >> 4;
    u32 mask[4];
#pragma unroll
    for (int q = 0; q < 4; ++q) mask[q] = (q == ((cc & 7) >> 1)) ? ((cc & 1) ? 0xFFFF0000u : 0x0000FFFFu) : 0u;
    Tile pre[PB > 0 ? PB : 1];
#pragma unroll
    for (int k = 0; k < PB; ++k) {
      const int n = c + NCONS * k;
      if (ok) ok = lds_wait_ge<1>(ctl + C_FULL, (u32)(n + 1), ctl + C_ABORT);
      asm volatile("" ::: "memory");
      tile_read(pre[k], (const LAS unsigned char*)lds + L_RING + (n % NSLOT) * SLOT_B + lane * 16);
      LGKM0();
      ctl[C_DONE + c] = (u32)(n + 1);
    }
    int n = c + NCONS * PB;
#pragma unroll 1
    for (int p = 0; p < NPH; ++p) {
      const bool isA = ph_isA(p);
      const int s0i = ph_slot(p);
      if (ok) ok = lds_wait_ge<1>(ctl + C_QRDY, (u32)(s0i + (isA ? 2 : 1)), ctl + C_ABORT);
      asm volatile("" ::: "memory");
      const int qslot = s0i + (isA ? (cc >> 3) : 0);
      u32x4 qf[8];
#pragma unroll
      for (int s = 0; s < 8; ++s) qf[s] = *LDSP(const u32x4, L_Q + qslot * 512 + s * 64 + g * 16);
      Acc a;
      a.m = -INFINITY; a.l = 0.f; a.o0 = f32x4{0.f, 0.f, 0.f, 0.f}; a.o1 = f32x4{0.f, 0.f, 0.f, 0.f};
      if (p == 0) {
#pragma unroll
        for (int k = 0; k < PB; ++k) tile_math(pre[k], qf, mask, a);
      }
      u32 tag = 0, reg = 0;
      if (p == NPH - 1) { tag = ctl[C_TAG]; reg = __builtin_amdgcn_readfirstlane(ctl[C_REG]); }
      const int nend = (p + 1) * TPP;
#pragma unroll 1
      for (; n < nend; n += NCONS) {
        if (ok) ok = lds_wait_ge<CONS_SLEEP>(ctl + C_FULL, (u32)(n + 1), ctl + C_ABORT);
        asm volatile("" ::: "memory");
        Tile t;
        tile_read(t, (const LAS unsigned char*)lds + L_RING + (n % NSLOT) * SLOT_B + lane * 16);
        LGKM0();
        ctl[C_DONE + c] = (u32)(n + 1);
        tile_math(t, qf, mask, a);
      }
      const float lt = xrow16_sum(a.l);
      const int hd = cc >> 3;
      if (((NPH - 1 - p) & 1) == c) {
        LAS f32x4* r = LDSP(f32x4, L_MRG + c * MRG_B);
        r[lane] = a.o0; r[64 + lane] = a.o1;
        *LDSP(u32x2, L_MRG + c * MRG_B + 2048 + lane * 8) = u32x2{__float_as_uint(a.m), __float_as_uint(lt)};
        LGKM0();
        ctl[C_DUMP + c] = (u32)(p + 1);
      } else {
        if (ok) ok = lds_wait_ge<0>(ctl + C_DUMP + (1 - c), (u32)(p + 1), ctl + C_ABORT);
        asm volatile("" ::: "memory");
        const LAS f32x4* r = LDSP(const f32x4, L_MRG + (1 - c) * MRG_B);
        const f32x4 b0 = r[lane], b1 = r[64 + lane];
        const u32x2 ml2 = *LDSP(const u32x2, L_MRG + (1 - c) * MRG_B + 2048 + lane * 8);
        const float m2 = __uint_as_float(ml2[0]), M = fmaxf(a.m, m2);
        const float w1 = __builtin_amdgcn_exp2f(a.m - M), w2 = __builtin_amdgcn_exp2f(m2 - M);
        const f32x4 x0 = w1 * a.o0 + w2 * b0, x1 = w1 * a.o1 + w2 * b1;
        const float L = fmaf(w1, lt, w2 * __uint_as_float(ml2[1]));
        const u32x2 r0 = {pack_bf16(x0[0], x0[1]), pack_bf16(x0[2], x0[3])}, r1 = {pack_bf16(x1[0], x1[1]), pack_bf16(x1[2], x1[3])};
        if (p < NPH - 1) {
          if (isA || hd == 0) {
            const int qo = L_Q + (s0i + hd) * 512 + g * 64 + (cc & 7) * 8;
            *LDSP(u32x2, qo) = r0;
            *LDSP(u32x2, qo + 256) = r1;
            if (g == 0 && (cc & 7) == 0) *LDSP(u32x2, L_ML + (s0i + hd) * 8) = u32x2{__float_as_uint(M), __float_as_uint(L)};
          }
          LGKM0();
          ctl[C_MERGED] = (u32)(p + 1);
        } else {
          const __amdgpu_buffer_rsrc_t rs = __builtin_amdgcn_make_buffer_rsrc(P.ws + W_REG + (size_t)reg * REG_B, 0, REG_B, 0x00020000);
          const int qh = slot_qh(NQS - 1, grp);
          const u32 o = R_PART + ((qh * 8 + g) * WPG + rank) * 128 + (cc & 7) * 16;
          ST_SC1((u32x4{r0[0], tag, r0[1], tag}), rs, hd == 0 ? o : OOB);
          ST_SC1((u32x4{r1[0], tag, r1[1], tag}), rs, hd == 0 ? o + 4 * WPG * 128 : OOB);
          ST_SC1((u32x4{__float_as_uint(M), tag, __float_as_uint(L), tag}), rs, lane == 0 ? R_ML + (qh * WPG + rank) * 16 : OOB);
          ctl[C_PUB] = 1u;
        }
      }
    }
    {
      const u32 tag = ctl[C_TAG];
      const __amdgpu_buffer_rsrc_t rs = __builtin_amdgcn_make_buffer_rsrc(P.ws + W_REG + (size_t)__builtin_amdgcn_readfirstlane(ctl[C_REG]) * REG_B, 0, REG_B, 0x00020000);
      HELP_GATHER(wave);
    }
  } else {
    const u32 tsvc = (u32)rtime();
    GAS u64* epoch_p = (GAS u64*)(P.ws + W_EPOCH);
    const u64 e0 = __hip_atomic_load(epoch_p, RLX_AGENT);
    const int prob = wg >> 7, w7 = wg & 127;
    const int sl = ((w7 >> 4) << 4) + ((w7 & 7) << 1) + ((w7 >> 3) & 1);
    const int a = lane >> 2, ch = lane & 3;
    const unsigned char* wq = (const unsigned char*)(prob ? P.wqB : P.wqA) + (size_t)(28 * a) * 8192 + sl * 64 + ch * 16;
    const unsigned char* xp = (const unsigned char*)(prob ? P.xB : P.xA) + a * 56;
    u32x4 wv[28];
    u32x2 xv[7];
#pragma unroll
    for (int i = 0; i < 7; ++i) xv[i] = __builtin_nontemporal_load((const GAS u32x2*)(xp + i * 8));
#pragma unroll
    for (int i = 0; i < 28; ++i) wv[i] = ld_nt(wq + (size_t)i * 8192);
    const u32 tag = (u32)(mix64(e0) >> 32);
    const u32 reg = __builtin_amdgcn_readfirstlane((u32)e0 & (NREG - 1));
    ctl[C_TAG] = tag; ctl[C_REG] = reg;
    const __amdgpu_buffer_rsrc_t rs = __builtin_amdgcn_make_buffer_rsrc(P.ws + W_REG + (size_t)reg * REG_B, 0, REG_B, 0x00020000);
    float acc[8];
#pragma unroll
    for (int k = 0; k < 8; ++k) acc[k] = 0.f;
#pragma unroll
    for (int i = 0; i < 14; ++i) {
      const u32 xx = xv[i >> 1][i & 1];
#pragma unroll
      for (int d = 0; d < 4; ++d) {
        const u32 w0 = wv[2 * i][d], w1 = wv[2 * i + 1][d];
        acc[2 * d] = dot2(__builtin_amdgcn_perm(w1, w0, 0x05040100u), xx, acc[2 * d]);
        acc[2 * d + 1] = dot2(__builtin_amdgcn_perm(w1, w0, 0x07060302u), xx, acc[2 * d + 1]);
      }
    }
#pragma unroll
    for (int k = 0; k < 8; ++k) {
      float v = acc[k];
      v += dpp<D_ROR4>(v); v += dpp<D_ROR8>(v);
      acc[k] = xrow16_sum(v);
    }
    {
      u32 pk = pack_bf16(acc[0], acc[1]);
#pragma unroll
      for (int k = 1; k < 4; ++k) pk = (a == k) ? pack_bf16(acc[2 * k], acc[2 * k + 1]) : pk;
      if (a < 4) __hip_atomic_store((GAS u64*)(P.ws + W_REG + (size_t)reg * REG_B + R_QG) + prob * 2048 + sl * 16 + ch * 4 + a, ((u64)tag << 32) | pk, RLX_AGENT);
    }
    {
      u32x4 v;
      const u64 t0 = rtime();
      for (u32 sp = 1; ok; ++sp) {
        v = LD_SC1(rs, R_QG + slot_qh(0, grp) * 1024 + lane * 16);
        if (__all(tagged(v, tag))) break;
        __builtin_amdgcn_s_sleep(4);
        if (poll_expired(sp, t0, ctl + C_ABORT)) ok = false;
      }
      *LDSP(u32x2, L_Q + lane * 8) = u32x2{v[0], v[2]};
      LGKM0();
      ctl[C_QRDY] = 1u;
    }
    {
      constexpr int QB = (NQS - 1) < 16 ? (NQS - 1) : 16;
#pragma unroll 1
      for (int q0 = 1; q0 < NQS; q0 += QB) {
        u32x4 v[QB];
        const u64 t0 = rtime();
        for (u32 sp = 1; ok; ++sp) {
          bool good = true;
#pragma unroll
          for (int i = 0; i < QB; ++i) {
            const int qs = (q0 + i < NQS) ? q0 + i : NQS - 1;
            v[i] = LD_SC1(rs, R_QG + slot_qh(qs, grp) * 1024 + lane * 16);
          }
#pragma unroll
          for (int i = 0; i < QB; ++i) good &= tagged(v[i], tag);
          if (__all(good)) break;
          __builtin_amdgcn_s_sleep(8);
          if (poll_expired(sp, t0, ctl + C_ABORT)) ok = false;
        }
#pragma unroll
        for (int i = 0; i < QB; ++i)
          if (q0 + i < NQS) *LDSP(u32x2, L_Q + (q0 + i) * 512 + lane * 8) = u32x2{v[i][0], v[i][2]};
        LGKM0();
        ctl[C_QRDY] = (u32)((q0 + QB < NQS) ? q0 + QB : NQS);
      }
    }
    const int uq = wg >> 3, us = wg & 7;
    u32 wo[32][4];
    {
      const unsigned char* wp = (const unsigned char*)(uq >= 16 ? P.woB : P.woA) + (size_t)((uq & 15) * 256 + us * 32) * 896 + lane * 4;
#pragma unroll
      for (int k = 0; k < 32; ++k)
#pragma unroll
        for (int i = 0; i < 4; ++i)
          wo[k][i] = __builtin_nontemporal_load((const GAS u32*)(wp + (8 * (k & 3) + (k >> 2)) * 896 + ((i < 3 || lane < 32) ? i * 256 : 0)));
#pragma unroll
      for (int kp = 0; kp < 16; ++kp)
#pragma unroll
        for (int i = 0; i < 4; ++i) {
          const u32 w0 = wo[2 * kp][i], w1 = wo[2 * kp + 1][i];
          wo[2 * kp][i] = __builtin_amdgcn_perm(w1, w0, 0x05040100u);
          wo[2 * kp + 1][i] = __builtin_amdgcn_perm(w1, w0, 0x07060302u);
          asm volatile("" : "+v"(wo[2 * kp][i]), "+v"(wo[2 * kp + 1][i]));
        }
    }
    if (ok) ok = lds_wait_ge<16>(ctl + C_MERGED, (u32)(NPH - 1), ctl + C_ABORT);
    while (ctl[C_FULL] < (u32)(NT - OLD_LEAD) && (!PACE || (int)((u32)rtime() - tsvc) < (((NT - BURST) * PACE) >> 8) + OLD_GATE) && ctl[C_ABORT] == 0u) __builtin_amdgcn_s_sleep(4);
    asm volatile("" ::: "memory");
#pragma unroll 4
    for (int qs = 0; qs < NQS - 1; ++qs) {
      const u32x2 v = *LDSP(const u32x2, L_Q + qs * 512 + lane * 8);
      ST_SC1((u32x4{v[0], tag, v[1], tag}), rs, R_PART + ((slot_qh(qs, grp) * 8 + (lane >> 3)) * WPG + rank) * 128 + (lane & 7) * 16);
    }
    {
      const int qs = lane < NQS - 1 ? lane : 0;
      const u32x2 v = *LDSP(const u32x2, L_ML + qs * 8);
      ST_SC1((u32x4{v[0], tag, v[1], tag}), rs, lane < NQS - 1 ? R_ML + (slot_qh(qs, grp) * WPG + rank) * 16 : OOB);
    }
    if (ok) ok = lds_wait_ge<0>(ctl + C_PUB, 1u, ctl + C_ABORT);
    pause(DELAY1);
    POLL_WAIT();
    float M, L, o[4];
    ok = gather_part(rs, tag, uq, us, NGW == 4 ? 3 * NPW : 0, lane, ok, ctl + C_ABORT, M, L, o);
    if (NGW == 4) {
      if (ok) ok = lds_wait_ge<0>(ctl + C_GCNT, 3u, ctl + C_ABORT);
      asm volatile("" ::: "memory");
      const LAS float* wp = LDSP(const float, L_WP);
      float Mt = M;
#pragma unroll
      for (int w = 0; w < 3; ++w) Mt = fmaxf(Mt, wp[w * 40 + 32]);
      const float f3 = __builtin_amdgcn_exp2f(M - Mt);
      L *= f3;
#pragma unroll
      for (int k = 0; k < 4; ++k) o[k] *= f3;
#pragma unroll
      for (int w = 0; w < 3; ++w) {
        const float f = __builtin_amdgcn_exp2f(wp[w * 40 + 32] - Mt);
        const f32x4 ow = *(const LAS f32x4*)(wp + w * 40 + (lane & 7) * 4);
        L = fmaf(f, wp[w * 40 + 33], L);
#pragma unroll
        for (int k = 0; k < 4; ++k) o[k] = fmaf(f, ow[k], o[k]);
      }
    }
    {
      const float inv = 1.f / L;
      const u32 op0 = pack_bf16(o[0] * inv, o[1] * inv), op1 = pack_bf16(o[2] * inv, o[3] * inv);
      float y[4][2];
#pragma unroll
      for (int i = 0; i < 4; ++i) { y[i][0] = 0.f; y[i][1] = 0.f; }
#pragma unroll
      for (int k = 0; k < 16; ++k) {
        const u32 ov = __builtin_amdgcn_readlane((k & 1) ? op1 : op0, k >> 1);
#pragma unroll
        for (int i = 0; i < 4; ++i) {
          y[i][0] = dot2(wo[2 * k][i], ov, y[i][0]);
          y[i][1] = dot2(wo[2 * k + 1][i], ov, y[i][1]);
        }
      }
      const u32 yo = R_Y + (((uq >> 4) * 28 + (lane >> 3)) * 128 + (wg & 127)) * 128 + (lane & 7) * 16;
#pragma unroll
      for (int i = 0; i < 4; ++i)
        ST_SC1((u32x4{__float_as_uint(y[i][0]), tag, __float_as_uint(y[i][1]), tag}), rs, (i < 3 || lane < 32) ? yo + i * 8 * 128 * 128 : OOB);
    }
    if (wg < 56) {
      ctl[C_YPUB] = 1u;
      pause(DELAY2);
      float s0, s1;
      ok = final_share(rs, tag, wg, FW - 1, lane, ok, ctl + C_ABORT, s0, s1);
      if (FW == 4) {
        if (ok) ok = lds_wait_ge<0>(ctl + C_FCNT, 3u, ctl + C_ABORT);
        asm volatile("" ::: "memory");
        const u32x2 v0 = *LDSP(const u32x2, L_FP + (lane & 7) * 8), v1 = *LDSP(const u32x2, L_FP + 64 + (lane & 7) * 8), v2 = *LDSP(const u32x2, L_FP + 128 + (lane & 7) * 8);
        s0 = ((__uint_as_float(v0[0]) + __uint_as_float(v1[0])) + __uint_as_float(v2[0])) + s0;
        s1 = ((__uint_as_float(v0[1]) + __uint_as_float(v1[1])) + __uint_as_float(v2[1])) + s1;
      }
      if (lane < 8) __hip_atomic_store((GAS u32*)(P.out + (wg / 28) * DM + (wg % 28) * 16 + lane * 2), pack_bf16(s0, s1), RLX_AGENT);
      if (wg == 28 && lane == 0) __hip_atomic_store(epoch_p, e0 + 1, RLX_AGENT);
    }
  }
}

extern "C" void kernel_launch(void* const* d_in, const int* in_sizes, int n_in, void* d_out, int out_size, void* d_ws, size_t ws_size,
                              hipStream_t stream) {
  (void)in_sizes; (void)n_in; (void)out_size;
  Params p;
  p.xA = (const unsigned short*)d_in[0]; p.kA = (const unsigned short*)d_in[1]; p.vA = (const unsigned short*)d_in[2];
  p.wqA = (const unsigned short*)d_in[3]; p.woA = (const unsigned short*)d_in[4];
  p.xB = (const unsigned short*)d_in[5]; p.kB = (const unsigned short*)d_in[6]; p.vB = (const unsigned short*)d_in[7];
  p.wqB = (const unsigned short*)d_in[8]; p.woB = (const unsigned short*)d_in[9];
  p.out = (unsigned short*)d_out;
  p.ws = (unsigned char*)d_ws + ((ws_size - WS_BYTES) & ~(size_t)4095);
  (void)hipFuncSetAttribute((const void*)fused_decode_kernel, hipFuncAttributeMaxDynamicSharedMemorySize, L_END);
  hipLaunchKernelGGL(fused_decode_kernel, dim3(NWG), dim3(NTHREADS), L_END, stream, p);
}
```
